```python
import torch

def _softmax(x, axis=-1):
    return torch.softmax(x, dim=axis)
def _gelu(x):
    return torch.nn.functional.gelu(x, approximate="tanh")
def _silu(x):
    return torch.nn.functional.silu(x)
def _sigmoid(x):
    return torch.sigmoid(x)
def _layernorm(x, axis=-1, eps=1e-5):
    m = x.mean(dim=axis, keepdim=True)
    v = x.var(dim=axis, keepdim=True, unbiased=False)
    return (x - m) / torch.sqrt(v + eps)
def _scatter_add(tgt, idx, src, axis=0):
    return tgt.clone().index_add_(axis, idx, src)
def _segment_sum(src, seg, n):
    out = torch.zeros((n,) + src.shape[1:], dtype=src.dtype)
    return out.index_add_(0, seg, src)
def _pad(x, pw):
    flat = tuple(v for pair in reversed(pw) for v in pair)
    return torch.nn.functional.pad(x, flat)

kernel_name = 'sequential_preLN__linear_kernel_hedgehog__dense_activation_silu__prompt'

def setup_inputs():

    _w = (lambda a: (lambda t: t.to(torch.bfloat16) if t.is_floating_point() else t)(torch.as_tensor(a)))
    def _A_setup():
        B, S, D, H = 1, 2048, 640, 32
        Dh = D // H
        return {
            "x": _w((lambda *s: torch.randn(*s, dtype=torch.bfloat16) * 0.05)(B, S, D)),
            "wqkv": _w((lambda *s: torch.randn(*s, dtype=torch.bfloat16) * 0.2)(D, 3 * D)),
            "w_phi": _w((lambda *s: torch.randn(*s, dtype=torch.bfloat16) * 0.2)(H, Dh, Dh) * (Dh ** -0.5)),
            "wo": _w((lambda *s: torch.randn(*s, dtype=torch.bfloat16) * 0.05)(D, D)),
        }
    _A_in = _A_setup()

    def _B_setup():
        B, S, D, F = 1, 2048, 640, 1728
        return {
            "x": _w((lambda *s: torch.randn(*s, dtype=torch.bfloat16) * 0.05)(B, S, D)),
            "w1": _w((lambda *s: torch.randn(*s, dtype=torch.bfloat16) * 1.6)(D, F) * (D ** -0.5)),
            "w2": _w((lambda *s: torch.randn(*s, dtype=torch.bfloat16) * 0.2)(F, D) * (F ** -0.5)),
        }
    _B_in = _B_setup()

    return {
        "A_x": _A_in["x"],
        "A_wqkv": _A_in["wqkv"],
        "A_w_phi": _A_in["w_phi"],
        "A_wo": _A_in["wo"],
        "B_x": _B_in["x"],
        "B_w1": _B_in["w1"],
        "B_w2": _B_in["w2"],
    }

def reference(A_x, A_wqkv, A_w_phi, A_wo, B_x, B_w1, B_w2):
    def _A_ref(x, wqkv, w_phi, wo):
        B, S, D = x.shape
        H = 32; Dh = D // H
        qkv = (lambda x, *d: x.permute(*d))((x @ wqkv).reshape(B, S, 3, H, Dh), 2, 0, 3, 1, 4)
        q, k, v = qkv[0], qkv[1], qkv[2]
        pq = torch.einsum("bhsd,hdf->bhsf", q, w_phi)
        pk = torch.einsum("bhsd,hdf->bhsf", k, w_phi)
        phi_q = torch.concatenate([_softmax(pq, axis=-1), _softmax(-pq, axis=-1)], axis=-1)
        phi_k = torch.concatenate([_softmax(pk, axis=-1), _softmax(-pk, axis=-1)], axis=-1)
        kv = torch.einsum("bhsf,bhsd->bhfd", phi_k, v)
        z  = torch.einsum("bhsf->bhf", phi_k)
        num = torch.einsum("bhqf,bhfd->bhqd", phi_q, kv)
        den = torch.einsum("bhqf,bhf->bhq", phi_q, z)[..., None] + 1e-6
        o = num / den
        return (lambda x, *d: x.permute(*d))(o, 0, 2, 1, 3).reshape(B, S, D) @ wo

    def _B_ref(x, w1, w2):
        h = torch.einsum("bsd,df->bsf", x, w1)
        h = _silu(h)
        out = torch.einsum("bsf,fd->bsd", h, w2)
        return out

    x = A_x
    h = x + _A_ref(_layernorm(x, axis=-1), A_wqkv, A_w_phi, A_wo)
    h2 = h + _B_ref(_layernorm(h, axis=-1), B_w1, B_w2)
    return h2
```

```cpp
#include <hip/hip_runtime.h>
#include <hip/hip_bf16.h>
#include <stdint.h>

typedef __attribute__((ext_vector_type(8))) short bf16x8;
typedef __attribute__((ext_vector_type(4))) short bf16x4;
typedef __attribute__((ext_vector_type(4))) float f32x4;
typedef __attribute__((ext_vector_type(4))) unsigned u32x4;
typedef unsigned short u16;

#define LDS_PTR(T, p) ((__attribute__((address_space(3))) T*)(p))

constexpr int S_ = 2048, D_ = 640, H_ = 32, DH_ = 20, F_ = 1728;

__device__ __forceinline__ float bf2f(u16 v) { return __uint_as_float(((unsigned)v) << 16); }
__device__ __forceinline__ u16 f2bf(float f) {
  __bf16 b = (__bf16)f;
  return __builtin_bit_cast(u16, b);
}
__device__ __forceinline__ float rbf(float f) { return bf2f(f2bf(f)); }
__device__ __forceinline__ unsigned pack2(float a, float b) {
  return (unsigned)f2bf(a) | ((unsigned)f2bf(b) << 16);
}

__device__ __forceinline__ bf16x4 tr_read(const char* lds_base, unsigned off) {
  return __builtin_amdgcn_ds_read_tr16_b64_v4i16(LDS_PTR(bf16x4, lds_base + off));
}

__device__ __forceinline__ unsigned bswz(unsigned r) { return (r & 3) | (((r >> 3) & 1) << 2); }

constexpr int K1_A_BYTES = 64 * 1280;
constexpr int K1_B_BYTES = 64 * 512;
constexpr int K1_LDS = K1_A_BYTES + 2 * K1_B_BYTES;

__global__ __launch_bounds__(256) void k1_qkv(const u16* __restrict__ x, const u16* __restrict__ wqkv,
                                              const u16* __restrict__ wphi, u16* __restrict__ phiq,
                                              float* __restrict__ kvp) {
  __shared__ __attribute__((aligned(16))) char smem[K1_LDS];
  char* sA = smem;
  char* sB = smem + K1_A_BYTES;
  const int tid = threadIdx.x, lane = tid & 63, w = tid >> 6;
  const int hg = blockIdx.x & 7, mt = blockIdx.x >> 3;
  const int m0 = mt * 64;

  u32x4 breg[8];
  auto loadB = [&](int t) {
#pragma unroll
    for (int j = 0; j < 8; ++j) {
      int i = tid + 256 * j;
      if (i < 1920) {
        int r = i / 30, n8 = i % 30;
        int part = n8 / 10, c = n8 % 10;
        const u16* g = wqkv + (size_t)(t * 64 + r) * 1920 + part * 640 + hg * 80 + c * 8;
        breg[j] = *(const u32x4*)g;
      }
    }
  };
  auto storeB = [&](int buf) {
#pragma unroll
    for (int j = 0; j < 8; ++j) {
      int i = tid + 256 * j;
      if (i < 1920) {
        int r = i / 30, n8 = i % 30;
        unsigned off = r * 512 + (((n8 >> 1) ^ bswz(r)) * 32) + (n8 & 1) * 16;
        *(u32x4*)(sB + buf * K1_B_BYTES + off) = breg[j];
      }
    }
  };
  loadB(0);

  {
    const int sub = lane >> 4, j = lane & 15;
#pragma unroll 1
    for (int pass = 0; pass < 4; ++pass) {
      const int row = w * 16 + pass * 4 + sub;
      const u16* g = x + (size_t)(m0 + row) * 640;
      u32x4 v[5];
#pragma unroll
      for (int i = 0; i < 5; ++i) v[i] = *(const u32x4*)(g + (j + 16 * i) * 8);
      float f[40];
#pragma unroll
      for (int i = 0; i < 5; ++i)
#pragma unroll
        for (int e = 0; e < 4; ++e) {
          f[i * 8 + e * 2] = __uint_as_float(v[i][e] << 16);
          f[i * 8 + e * 2 + 1] = __uint_as_float(v[i][e] & 0xffff0000u);
        }
      float s = 0.f;
#pragma unroll
      for (int e = 0; e < 40; ++e) s += f[e];
      s += __shfl_xor(s, 1); s += __shfl_xor(s, 2); s += __shfl_xor(s, 4); s += __shfl_xor(s, 8);
      const float mean = s * (1.f / 640.f);
      float q = 0.f;
#pragma unroll
      for (int e = 0; e < 40; ++e) { float d = f[e] - mean; q += d * d; }
      q += __shfl_xor(q, 1); q += __shfl_xor(q, 2); q += __shfl_xor(q, 4); q += __shfl_xor(q, 8);
      const float rstd = rsqrtf(q * (1.f / 640.f) + 1e-5f);
#pragma unroll
      for (int i = 0; i < 5; ++i) {
        u32x4 o;
#pragma unroll
        for (int e = 0; e < 4; ++e)
          o[e] = pack2((f[i * 8 + e * 2] - mean) * rstd, (f[i * 8 + e * 2 + 1] - mean) * rstd);
        const int c = j + 16 * i;
        *(u32x4*)(sA + row * 1280 + ((c ^ (row & 15)) * 16)) = o;
      }
    }
  }
  storeB(0);
  __syncthreads();

  const int wm = w >> 1, wn = w & 1;
  const int nt0 = wn * 8, ncnt = wn ? 7 : 8;
  f32x4 acc[2][8];
#pragma unroll
  for (int a = 0; a < 2; ++a)
#pragma unroll
    for (int b = 0; b < 8; ++b) acc[a][b] = (f32x4){0.f, 0.f, 0.f, 0.f};

  const int g4 = lane >> 4, q4 = (lane & 15) >> 2, p4 = lane & 3;
  for (int t = 0; t < 10; ++t) {
    if (t + 1 < 10) loadB(t + 1);
    const char* sBc = sB + (t & 1) * K1_B_BYTES;
#pragma unroll
    for (int kk = 0; kk < 2; ++kk) {
      bf16x8 af[2];
#pragma unroll
      for (int mi = 0; mi < 2; ++mi) {
        const int row = wm * 32 + mi * 16 + (lane & 15);
        const int c = t * 8 + kk * 4 + g4;
        af[mi] = *(const bf16x8*)(sA + row * 1280 + ((c ^ (row & 15)) * 16));
      }
      const int r0 = kk * 32 + 8 * g4 + q4;
      const unsigned fz = q4 | ((g4 & 1) << 2);
#pragma unroll
      for (int ni = 0; ni < 8; ++ni) {
        if (ni < ncnt) {
          const unsigned u = ((nt0 + ni) ^ fz) * 32 + 8 * p4;
          bf16x4 b0 = tr_read(sBc, r0 * 512 + u);
          bf16x4 b1 = tr_read(sBc, (r0 + 4) * 512 + u);
          bf16x8 bfr = __builtin_shufflevector(b0, b1, 0, 1, 2, 3, 4, 5, 6, 7);
#pragma unroll
          for (int mi = 0; mi < 2; ++mi)
            acc[mi][ni] = __builtin_amdgcn_mfma_f32_16x16x32_bf16(af[mi], bfr, acc[mi][ni], 0, 0, 0);
        }
      }
    }
    if (t + 1 < 10) storeB((t + 1) & 1);
    __syncthreads();
  }

  float* sQ = (float*)smem;
  float* sW = (float*)(smem + 64 * 244 * 4);
  float* sPK = sW + 1600;
#pragma unroll
  for (int mi = 0; mi < 2; ++mi)
#pragma unroll
    for (int ni = 0; ni < 8; ++ni)
      if (ni < ncnt) {
#pragma unroll
        for (int j = 0; j < 4; ++j) {
          const int row = wm * 32 + mi * 16 + g4 * 4 + j;
          const int col = (nt0 + ni) * 16 + (lane & 15);
          sQ[row * 244 + col] = rbf(acc[mi][ni][j]);
        }
      }
  for (int i = tid; i < 1600; i += 256) sW[i] = bf2f(wphi[(size_t)hg * 1600 + i]);
  __syncthreads();

  {
    const int row = tid >> 2, hh = tid & 3;
    const float* wp = sW + hh * 400;
#pragma unroll 1
    for (int part = 0; part < 2; ++part) {
      float qv[20];
#pragma unroll
      for (int d = 0; d < 20; ++d) qv[d] = sQ[row * 244 + part * 80 + hh * 20 + d];
      float pq[20];
#pragma unroll 2
      for (int f = 0; f < 20; ++f) {
        float s = 0.f;
#pragma unroll
        for (int d = 0; d < 20; ++d) s += qv[d] * wp[d * 20 + f];
        pq[f] = rbf(s);
      }
      float mx = pq[0], mn = pq[0];
#pragma unroll
      for (int f = 1; f < 20; ++f) { mx = fmaxf(mx, pq[f]); mn = fminf(mn, pq[f]); }
      float ep[20], en[20], sp = 0.f, sn = 0.f;
#pragma unroll
      for (int f = 0; f < 20; ++f) {
        ep[f] = __expf(pq[f] - mx); sp += ep[f];
        en[f] = __expf(mn - pq[f]); sn += en[f];
      }
      const float ip = 1.f / sp, in_ = 1.f / sn;
      if (part == 0) {
        unsigned o[20];
#pragma unroll
        for (int f = 0; f < 10; ++f) {
          o[f] = pack2(ep[2 * f] * ip, ep[2 * f + 1] * ip);
          o[10 + f] = pack2(en[2 * f] * in_, en[2 * f + 1] * in_);
        }
        u16* gp = phiq + ((size_t)(hg * 4 + hh) * S_ + m0 + row) * 40;
#pragma unroll
        for (int i = 0; i < 5; ++i) {
          u32x4 v = {o[4 * i], o[4 * i + 1], o[4 * i + 2], o[4 * i + 3]};
          *(u32x4*)(gp + i * 8) = v;
        }
      } else {
        float* pk = sPK + (row * 4 + hh) * 40;
#pragma unroll
        for (int f = 0; f < 20; ++f) { pk[f] = rbf(ep[f] * ip); pk[20 + f] = rbf(en[f] * in_); }
      }
    }
  }
  __syncthreads();
  if (tid < 160) {
    const int hh = tid / 40, f = tid % 40;
    float a[21];
#pragma unroll
    for (int d = 0; d < 21; ++d) a[d] = 0.f;
    for (int r = 0; r < 64; ++r) {
      const float pv = sPK[(r * 4 + hh) * 40 + f];
      const float* vv = sQ + r * 244 + 160 + hh * 20;
#pragma unroll
      for (int d = 0; d < 20; ++d) a[d] += pv * vv[d];
      a[20] += pv;
    }
    float* o = kvp + ((size_t)(hg * 4 + hh) * 32 + mt) * 840 + f * 21;
#pragma unroll
    for (int d = 0; d < 21; ++d) o[d] = a[d];
  }
}

__global__ __launch_bounds__(256) void k15_o(const u16* __restrict__ phiq, const float* __restrict__ kvp,
                                             u16* __restrict__ o) {
  __shared__ float skv[840];
  const int head = blockIdx.x >> 3, rc = blockIdx.x & 7;
  const int tid = threadIdx.x;
  for (int i = tid; i < 840; i += 256) {
    float s = 0.f;
    const float* p = kvp + (size_t)head * 32 * 840 + i;
#pragma unroll 8
    for (int m = 0; m < 32; ++m) s += p[m * 840];
    skv[i] = rbf(s);
  }
  __syncthreads();
  const int row = rc * 256 + tid;
  const u16* gp = phiq + ((size_t)head * S_ + row) * 40;
  float ph[40];
#pragma unroll
  for (int i = 0; i < 5; ++i) {
    u32x4 v = *(const u32x4*)(gp + i * 8);
#pragma unroll
    for (int e = 0; e < 4; ++e) {
      ph[i * 8 + 2 * e] = __uint_as_float(v[e] << 16);
      ph[i * 8 + 2 * e + 1] = __uint_as_float(v[e] & 0xffff0000u);
    }
  }
  float num[20], den = 0.f;
#pragma unroll
  for (int d = 0; d < 20; ++d) num[d] = 0.f;
#pragma unroll
  for (int f = 0; f < 40; ++f) {
#pragma unroll
    for (int d = 0; d < 20; ++d) num[d] += ph[f] * skv[f * 21 + d];
    den += ph[f] * skv[f * 21 + 20];
  }
  den = rbf(rbf(den) + 1e-6f);
  unsigned ov[10];
#pragma unroll
  for (int d = 0; d < 10; ++d) ov[d] = pack2(rbf(num[2 * d]) / den, rbf(num[2 * d + 1]) / den);
  unsigned* op = (unsigned*)(o + (size_t)row * 640 + head * 20);
#pragma unroll
  for (int d = 0; d < 10; ++d) op[d] = ov[d];
}

template <int KTOT>
__global__ __launch_bounds__(256) void k_gemm64x80(const u16* __restrict__ A, const u16* __restrict__ B,
                                                   const u16* __restrict__ res, u16* __restrict__ out) {
  constexpr int NT = KTOT / 64;
  __shared__ __attribute__((aligned(16))) char smem[2 * 8192 + 2 * 16384];
  char* sA = smem;
  char* sB = smem + 16384;
  const int tid = threadIdx.x, lane = tid & 63, w = tid >> 6;
  const int nt = blockIdx.x & 7, mt = blockIdx.x >> 3;
  const int m0 = mt * 64, n0 = nt * 80;

  u32x4 areg[2], breg[3];
  auto loadT = [&](int t) {
#pragma unroll
    for (int j = 0; j < 2; ++j) {
      int i = tid + 256 * j;
      int r = i >> 3, c = i & 7;
      areg[j] = *(const u32x4*)(A + (size_t)(m0 + r) * KTOT + t * 64 + c * 8);
    }
#pragma unroll
    for (int j = 0; j < 3; ++j) {
      int i = tid + 256 * j;
      if (i < 640) {
        int r = i / 10, c = i % 10;
        breg[j] = *(const u32x4*)(B + (size_t)(t * 64 + r) * 640 + n0 + c * 8);
      }
    }
  };
  auto storeT = [&](int buf) {
#pragma unroll
    for (int j = 0; j < 2; ++j) {
      int i = tid + 256 * j;
      int r = i >> 3, c = i & 7;
      *(u32x4*)(sA + buf * 8192 + r * 128 + ((c ^ (r & 7)) * 16)) = areg[j];
    }
#pragma unroll
    for (int j = 0; j < 3; ++j) {
      int i = tid + 256 * j;
      if (i < 640) {
        int r = i / 10, c = i % 10;
        *(u32x4*)(sB + buf * 16384 + r * 256 + (((c >> 1) ^ bswz(r)) * 32) + (c & 1) * 16) = breg[j];
      }
    }
  };
  loadT(0);
  storeT(0);
  __syncthreads();

  const int wm = w >> 1, wk = w & 1;
  f32x4 acc[2][5];
#pragma unroll
  for (int a = 0; a < 2; ++a)
#pragma unroll
    for (int b = 0; b < 5; ++b) acc[a][b] = (f32x4){0.f, 0.f, 0.f, 0.f};
  const int g4 = lane >> 4, q4 = (lane & 15) >> 2, p4 = lane & 3;
  for (int t = 0; t < NT; ++t) {
    if (t + 1 < NT) loadT(t + 1);
    const char* sAc = sA + (t & 1) * 8192;
    const char* sBc = sB + (t & 1) * 16384;
    bf16x8 af[2];
#pragma unroll
    for (int mi = 0; mi < 2; ++mi) {
      const int row = wm * 32 + mi * 16 + (lane & 15);
      const int c = wk * 4 + g4;
      af[mi] = *(const bf16x8*)(sAc + row * 128 + ((c ^ (row & 7)) * 16));
    }
    const int r0 = wk * 32 + 8 * g4 + q4;
    const unsigned fz = q4 | ((g4 & 1) << 2);
#pragma unroll
    for (int ni = 0; ni < 5; ++ni) {
      const unsigned u = (ni ^ fz) * 32 + 8 * p4;
      bf16x4 b0 = tr_read(sBc, r0 * 256 + u);
      bf16x4 b1 = tr_read(sBc, (r0 + 4) * 256 + u);
      bf16x8 bfr = __builtin_shufflevector(b0, b1, 0, 1, 2, 3, 4, 5, 6, 7);
#pragma unroll
      for (int mi = 0; mi < 2; ++mi)
        acc[mi][ni] = __builtin_amdgcn_mfma_f32_16x16x32_bf16(af[mi], bfr, acc[mi][ni], 0, 0, 0);
    }
    if (t + 1 < NT) storeT((t + 1) & 1);
    __syncthreads();
  }
  float* sR = (float*)smem;
  if (wk == 1) {
#pragma unroll
    for (int mi = 0; mi < 2; ++mi)
#pragma unroll
      for (int ni = 0; ni < 5; ++ni)
        *(f32x4*)(sR + (((wm * 2 + mi) * 5 + ni) * 64 + lane) * 4) = acc[mi][ni];
  }
  __syncthreads();
  if (wk == 0) {
#pragma unroll
    for (int mi = 0; mi < 2; ++mi)
#pragma unroll
      for (int ni = 0; ni < 5; ++ni) {
        f32x4 o = *(f32x4*)(sR + (((wm * 2 + mi) * 5 + ni) * 64 + lane) * 4);
#pragma unroll
        for (int j = 0; j < 4; ++j) {
          const int row = m0 + wm * 32 + mi * 16 + g4 * 4 + j;
          const int col = n0 + ni * 16 + (lane & 15);
          const float v = rbf(acc[mi][ni][j] + o[j]);
          out[(size_t)row * 640 + col] = f2bf(bf2f(res[(size_t)row * 640 + col]) + v);
        }
      }
  }
}

__global__ __launch_bounds__(256) void k3_ffn1(const u16* __restrict__ h, const u16* __restrict__ w1,
                                               u16* __restrict__ hmid) {
  __shared__ __attribute__((aligned(16))) char smem[2 * 16384 + 2 * 16384 + 1024];
  char* sA = smem;
  char* sB = smem + 32768;
  float* sMean = (float*)(smem + 65536);
  float* sRstd = sMean + 128;
  const int tid = threadIdx.x, lane = tid & 63, w = tid >> 6;
  const int nt = blockIdx.x % 14, mt = blockIdx.x / 14;
  const int m0 = mt * 128, n0 = nt * 128;

  u32x4 areg[4], breg[4];
  auto loadT = [&](int t) {
#pragma unroll
    for (int j = 0; j < 4; ++j) {
      int i = tid + 256 * j;
      int r = i >> 3, c = i & 7;
      areg[j] = *(const u32x4*)(h + (size_t)(m0 + r) * 640 + t * 64 + c * 8);
    }
#pragma unroll
    for (int j = 0; j < 4; ++j) {
      int i = tid + 256 * j;
      int r = i >> 4, c = i & 15;
      int col = n0 + c * 8;
      if (col >= F_) col = F_ - 8;
      breg[j] = *(const u32x4*)(w1 + (size_t)(t * 64 + r) * F_ + col);
    }
  };
  auto storeT = [&](int buf) {
#pragma unroll
    for (int j = 0; j < 4; ++j) {
      int i = tid + 256 * j;
      int r = i >> 3, c = i & 7;
      const float mean = sMean[r], rstd = sRstd[r];
      u32x4 o;
#pragma unroll
      for (int e = 0; e < 4; ++e)
        o[e] = pack2((__uint_as_float(areg[j][e] << 16) - mean) * rstd,
                     (__uint_as_float(areg[j][e] & 0xffff0000u) - mean) * rstd);
      *(u32x4*)(sA + buf * 16384 + r * 128 + ((c ^ (r & 7)) * 16)) = o;
    }
#pragma unroll
    for (int j = 0; j < 4; ++j) {
      int i = tid + 256 * j;
      int r = i >> 4, c = i & 15;
      *(u32x4*)(sB + buf * 16384 + r * 256 + (((c >> 1) ^ bswz(r)) * 32) + (c & 1) * 16) = breg[j];
    }
  };
  loadT(0);
  {
    const int sub = lane >> 4, j = lane & 15;
#pragma unroll 1
    for (int pass = 0; pass < 8; ++pass) {
      const int row = w * 32 + pass * 4 + sub;
      const u16* g = h + (size_t)(m0 + row) * 640;
      u32x4 v[5];
#pragma unroll
      for (int i = 0; i < 5; ++i) v[i] = *(const u32x4*)(g + (j + 16 * i) * 8);
      float f[40];
#pragma unroll
      for (int i = 0; i < 5; ++i)
#pragma unroll
        for (int e = 0; e < 4; ++e) {
          f[i * 8 + e * 2] = __uint_as_float(v[i][e] << 16);
          f[i * 8 + e * 2 + 1] = __uint_as_float(v[i][e] & 0xffff0000u);
        }
      float s = 0.f;
#pragma unroll
      for (int e = 0; e < 40; ++e) s += f[e];
      s += __shfl_xor(s, 1); s += __shfl_xor(s, 2); s += __shfl_xor(s, 4); s += __shfl_xor(s, 8);
      const float mean = s * (1.f / 640.f);
      float q = 0.f;
#pragma unroll
      for (int e = 0; e < 40; ++e) { float d = f[e] - mean; q += d * d; }
      q += __shfl_xor(q, 1); q += __shfl_xor(q, 2); q += __shfl_xor(q, 4); q += __shfl_xor(q, 8);
      if (j == 0) { sMean[row] = mean; sRstd[row] = rsqrtf(q * (1.f / 640.f) + 1e-5f); }
    }
  }
  __syncthreads();
  storeT(0);
  __syncthreads();

  const int wm = w >> 1, wn = w & 1;
  f32x4 acc[4][4];
#pragma unroll
  for (int a = 0; a < 4; ++a)
#pragma unroll
    for (int b = 0; b < 4; ++b) acc[a][b] = (f32x4){0.f, 0.f, 0.f, 0.f};
  const int g4 = lane >> 4, q4 = (lane & 15) >> 2, p4 = lane & 3;
  for (int t = 0; t < 10; ++t) {
    if (t + 1 < 10) loadT(t + 1);
    const char* sAc = sA + (t & 1) * 16384;
    const char* sBc = sB + (t & 1) * 16384;
#pragma unroll
    for (int kk = 0; kk < 2; ++kk) {
      bf16x8 af[4];
#pragma unroll
      for (int mi = 0; mi < 4; ++mi) {
        const int row = wm * 64 + mi * 16 + (lane & 15);
        const int c = kk * 4 + g4;
        af[mi] = *(const bf16x8*)(sAc + row * 128 + ((c ^ (row & 7)) * 16));
      }
      const int r0 = kk * 32 + 8 * g4 + q4;
      const unsigned fz = q4 | ((g4 & 1) << 2);
#pragma unroll
      for (int ni = 0; ni < 4; ++ni) {
        const unsigned u = ((wn * 4 + ni) ^ fz) * 32 + 8 * p4;
        bf16x4 b0 = tr_read(sBc, r0 * 256 + u);
        bf16x4 b1 = tr_read(sBc, (r0 + 4) * 256 + u);
        bf16x8 bfr = __builtin_shufflevector(b0, b1, 0, 1, 2, 3, 4, 5, 6, 7);
#pragma unroll
        for (int mi = 0; mi < 4; ++mi)
          acc[mi][ni] = __builtin_amdgcn_mfma_f32_16x16x32_bf16(af[mi], bfr, acc[mi][ni], 0, 0, 0);
      }
    }
    if (t + 1 < 10) storeT((t + 1) & 1);
    __syncthreads();
  }
#pragma unroll
  for (int mi = 0; mi < 4; ++mi)
#pragma unroll
    for (int ni = 0; ni < 4; ++ni) {
      const int col = n0 + wn * 64 + ni * 16 + (lane & 15);
      if (col < F_) {
#pragma unroll
        for (int j = 0; j < 4; ++j) {
          const int row = m0 + wm * 64 + mi * 16 + g4 * 4 + j;
          const float v = rbf(acc[mi][ni][j]);
          const float sg = v / (1.f + __expf(-v));
          hmid[(size_t)row * F_ + col] = f2bf(sg);
        }
      }
    }
}

extern "C" void kernel_launch(void* const* d_in, const int* in_sizes, int n_in, void* d_out, int out_size,
                              void* d_ws, size_t ws_size, hipStream_t stream) {
  const u16* x = (const u16*)d_in[0];
  const u16* wqkv = (const u16*)d_in[1];
  const u16* wphi = (const u16*)d_in[2];
  const u16* wo = (const u16*)d_in[3];
  const u16* w1 = (const u16*)d_in[5];
  const u16* w2 = (const u16*)d_in[6];
  u16* out = (u16*)d_out;
  char* ws = (char*)d_ws;
  u16* phiq = (u16*)ws;
  float* kvp = (float*)(ws + (8 << 20));
  u16* o = (u16*)(ws + (16 << 20));
  u16* h = (u16*)(ws + (24 << 20));
  u16* hmid = (u16*)(ws + (32 << 20));

  k1_qkv<<<256, 256, 0, stream>>>(x, wqkv, wphi, phiq, kvp);
  k15_o<<<256, 256, 0, stream>>>(phiq, kvp, o);
  k_gemm64x80<640><<<256, 256, 0, stream>>>(o, wo, x, h);
  k3_ffn1<<<16 * 14, 256, 0, stream>>>(h, w1, hmid);
  k_gemm64x80<1728><<<256, 256, 0, stream>>>(hmid, w2, h, out);
}
```

```cpp
#include <hip/hip_runtime.h>
#include <hip/hip_bf16.h>
#include <stdint.h>

typedef __attribute__((ext_vector_type(8))) short bf16x8;
typedef __attribute__((ext_vector_type(4))) short bf16x4;
typedef __attribute__((ext_vector_type(4))) float f32x4;
typedef __attribute__((ext_vector_type(4))) unsigned u32x4;
typedef __attribute__((ext_vector_type(2))) unsigned u32x2;
typedef unsigned short u16;

#define LDS_PTR(T, p) ((__attribute__((address_space(3))) T*)(p))
#define WAIT_V(n) asm volatile("s_waitcnt vmcnt(" #n ")" ::: "memory")
#define BAR() __builtin_amdgcn_s_barrier()
#define MFMA(a, b, c) __builtin_amdgcn_mfma_f32_16x16x32_bf16(a, b, c, 0, 0, 0)

constexpr int S_ = 2048, D_ = 640, H_ = 32, DH_ = 20, F_ = 1728;

__device__ __forceinline__ float bf2f(u16 v) { return __uint_as_float(((unsigned)v) << 16); }
__device__ __forceinline__ u16 f2bf(float f) {
  __bf16 b = (__bf16)f;
  return __builtin_bit_cast(u16, b);
}
__device__ __forceinline__ float rbf(float f) { return bf2f(f2bf(f)); }
__device__ __forceinline__ unsigned pack2(float a, float b) {
  return (unsigned)f2bf(a) | ((unsigned)f2bf(b) << 16);
}
__device__ __forceinline__ bf16x4 tr_read(const char* lds_base, unsigned off) {
  return __builtin_amdgcn_ds_read_tr16_b64_v4i16(LDS_PTR(bf16x4, lds_base + off));
}
__device__ __forceinline__ bf16x8 tr_read2(const char* lds_base, unsigned off, unsigned rowbytes4) {
  bf16x4 b0 = tr_read(lds_base, off);
  bf16x4 b1 = tr_read(lds_base, off + rowbytes4);
  return __builtin_shufflevector(b0, b1, 0, 1, 2, 3, 4, 5, 6, 7);
}
__device__ __forceinline__ void glds16(const u16* g, unsigned dst_uniform) {
  unsigned keep;
  asm volatile("s_mov_b32 %0, m0\n\ts_mov_b32 m0, %2\n\ts_nop 0\n\tglobal_load_lds_dwordx4 %1, off\n\ts_mov_b32 m0, %0"
               : "=&s"(keep) : "v"(g), "s"(dst_uniform) : "memory");
}
__device__ __forceinline__ unsigned lds_addr(const void* p) {
  return __builtin_amdgcn_readfirstlane((unsigned)(size_t)LDS_PTR(char, p));
}
__device__ __forceinline__ unsigned bswz(unsigned r) { return (r & 3) | (((r >> 3) & 1) << 2); }

__device__ __forceinline__ float red16_sum(float v) {
  v += __shfl_xor(v, 1); v += __shfl_xor(v, 2); v += __shfl_xor(v, 4); v += __shfl_xor(v, 8);
  return v;
}
__device__ __forceinline__ float red16_max(float v) {
  v = fmaxf(v, __shfl_xor(v, 1)); v = fmaxf(v, __shfl_xor(v, 2));
  v = fmaxf(v, __shfl_xor(v, 4)); v = fmaxf(v, __shfl_xor(v, 8));
  return v;
}

constexpr int K1_A_BYTES = 64 * 1280;
constexpr int K1_SLOT = 32 * 512;
constexpr int K1_LDS = K1_A_BYTES + 4 * K1_SLOT;

__global__ __launch_bounds__(512) void k1_qkv(const u16* __restrict__ x, const u16* __restrict__ wqkv,
                                              const u16* __restrict__ wphi, u16* __restrict__ phiq,
                                              float* __restrict__ kvp) {
  __shared__ __attribute__((aligned(16))) char smem[K1_LDS];
  char* sA = smem;
  char* sB = smem + K1_A_BYTES;
  const int tid = threadIdx.x, lane = tid & 63, w = tid >> 6;
  const int hg = blockIdx.x & 7, mt = blockIdx.x >> 3;
  const int m0 = mt * 64;

  int bsrc_off[2];
#pragma unroll
  for (int j = 0; j < 2; ++j) {
    const int p = w * 2 + j;
    const int r = p * 2 + (lane >> 5);
    const int pc = lane & 31;
    int u = (pc >> 1) ^ bswz(r);
    if (u == 15) u = 14;
    const int n8 = u * 2 + (pc & 1);
    const int part = n8 / 10, c = n8 % 10;
    bsrc_off[j] = r * 1920 + part * 640 + hg * 80 + c * 8;
  }
  const unsigned sB_u = lds_addr(sB);
  const int wu = __builtin_amdgcn_readfirstlane(w);
  auto issueB = [&](int t) {
    const u16* g = wqkv + (size_t)t * 32 * 1920;
    const unsigned d = sB_u + (t & 3) * K1_SLOT + (wu * 2) * 1024;
#pragma unroll
    for (int j = 0; j < 2; ++j) glds16(g + bsrc_off[j], d + j * 1024);
  };
  issueB(0); issueB(1); issueB(2);

  {
    const int sub = lane >> 4, j = lane & 15;
    u32x4 v[2][5];
#pragma unroll
    for (int pass = 0; pass < 2; ++pass) {
      const u16* g = x + (size_t)(m0 + w * 8 + pass * 4 + sub) * 640;
#pragma unroll
      for (int i = 0; i < 5; ++i) v[pass][i] = *(const u32x4*)(g + (j + 16 * i) * 8);
    }
#pragma unroll
    for (int pass = 0; pass < 2; ++pass) {
      const int row = w * 8 + pass * 4 + sub;
      float f[40];
#pragma unroll
      for (int i = 0; i < 5; ++i)
#pragma unroll
        for (int e = 0; e < 4; ++e) {
          f[i * 8 + e * 2] = __uint_as_float(v[pass][i][e] << 16);
          f[i * 8 + e * 2 + 1] = __uint_as_float(v[pass][i][e] & 0xffff0000u);
        }
      float s = 0.f;
#pragma unroll
      for (int e = 0; e < 40; ++e) s += f[e];
      s = red16_sum(s);
      const float mean = s * (1.f / 640.f);
      float q = 0.f;
#pragma unroll
      for (int e = 0; e < 40; ++e) { f[e] -= mean; q += f[e] * f[e]; }
      q = red16_sum(q);
      const float rstd = rsqrtf(q * (1.f / 640.f) + 1e-5f);
#pragma unroll
      for (int i = 0; i < 5; ++i) {
        u32x4 o;
#pragma unroll
        for (int e = 0; e < 4; ++e) o[e] = pack2(f[i * 8 + e * 2] * rstd, f[i * 8 + e * 2 + 1] * rstd);
        const int c = j + 16 * i;
        *(u32x4*)(sA + row * 1280 + ((c ^ (row & 15)) * 16)) = o;
      }
    }
  }

  const int wm = w >> 2, wn = w & 3;
  const int nt0 = wn * 4, ncnt = (wn == 3) ? 3 : 4;
  f32x4 acc[2][4];
#pragma unroll
  for (int a = 0; a < 2; ++a)
#pragma unroll
    for (int b = 0; b < 4; ++b) acc[a][b] = (f32x4){0.f, 0.f, 0.f, 0.f};

  const int g4 = lane >> 4, q4 = (lane & 15) >> 2, p4 = lane & 3;
  const unsigned fz = q4 | ((g4 & 1) << 2);
  const int r0 = 8 * g4 + q4;
  asm volatile("s_waitcnt lgkmcnt(0)" ::: "memory");
#pragma unroll 1
  for (int t = 0; t < 20; ++t) {
    if (t < 18) { WAIT_V(4); } else if (t == 18) { WAIT_V(2); } else { WAIT_V(0); }
    BAR();
    if (t + 3 < 20) issueB(t + 3);
    const char* sBc = sB + (t & 3) * K1_SLOT;
    bf16x8 af[2];
#pragma unroll
    for (int mi = 0; mi < 2; ++mi) {
      const int row = wm * 32 + mi * 16 + (lane & 15);
      const int c = t * 4 + g4;
      af[mi] = *(const bf16x8*)(sA + row * 1280 + ((c ^ (row & 15)) * 16));
    }
#pragma unroll
    for (int ni = 0; ni < 4; ++ni) {
      if (ni < ncnt) {
        bf16x8 bfr = tr_read2(sBc, r0 * 512 + ((nt0 + ni) ^ fz) * 32 + 8 * p4, 4 * 512);
#pragma unroll
        for (int mi = 0; mi < 2; ++mi) acc[mi][ni] = MFMA(af[mi], bfr, acc[mi][ni]);
      }
    }
  }
  __syncthreads();

  u16* sQ = (u16*)smem;
  u16* sPK = (u16*)(smem + 32768);
  u16* sV = (u16*)(smem + 57344);
  u16* sPQ = (u16*)(smem + 73728);
#pragma unroll
  for (int mi = 0; mi < 2; ++mi)
#pragma unroll
    for (int ni = 0; ni < 4; ++ni)
      if (ni < ncnt) {
#pragma unroll
        for (int j = 0; j < 4; ++j) {
          const int row = wm * 32 + mi * 16 + g4 * 4 + j;
          const int col = (nt0 + ni) * 16 + (lane & 15);
          const u16 b = f2bf(acc[mi][ni][j]);
          sQ[row * 256 + col] = b;
          if (col >= 160) {
            const int hv = (col - 160) / 20, dv = (col - 160) % 20;
            sV[(hv * 64 + row) * 32 + dv] = b;
          }
        }
      }
  if (tid < 256) {
    u16* vp = sV + tid * 32 + 20;
    *(u32x2*)(vp) = (u32x2){0x00003f80u, 0u};
    *(u32x4*)(vp + 4) = (u32x4){0u, 0u, 0u, 0u};
  }
  const int ph = w & 3, pp = w >> 2;
  bf16x8 wf[2];
  {
    const u16* wp = wphi + (size_t)(hg * 4 + ph) * 400;
#pragma unroll
    for (int nt = 0; nt < 2; ++nt) {
      const int f = nt * 16 + (lane & 15);
#pragma unroll
      for (int j = 0; j < 8; ++j) {
        const int d = 8 * g4 + j;
        wf[nt][j] = (d < 20 && f < 20) ? (short)wp[d * 20 + f] : (short)0;
      }
    }
  }
  __syncthreads();

#pragma unroll 1
  for (int mi = 0; mi < 4; ++mi) {
    const int arow = mi * 16 + (lane & 15);
    const u16* ap = sQ + arow * 256 + pp * 80 + ph * 20 + 8 * g4;
    u32x2 a0 = *(const u32x2*)ap;
    u32x2 a1 = *(const u32x2*)(ap + 4);
    u32x4 av = {a0[0], a0[1], a1[0], a1[1]};
    bf16x8 af = __builtin_bit_cast(bf16x8, av);
    f32x4 z = {0.f, 0.f, 0.f, 0.f};
    f32x4 p0 = MFMA(af, wf[0], z);
    f32x4 p1 = MFMA(af, wf[1], z);
    const bool v1 = (lane & 15) < 4;
#pragma unroll
    for (int j = 0; j < 4; ++j) {
      const float a = rbf(p0[j]);
      const float b = rbf(p1[j]);
      float mx = v1 ? fmaxf(a, b) : a;
      float mn = v1 ? fminf(a, b) : a;
      mx = red16_max(mx);
      mn = -red16_max(-mn);
      const float ea = __expf(a - mx), na = __expf(mn - a);
      const float eb = v1 ? __expf(b - mx) : 0.f, nb = v1 ? __expf(mn - b) : 0.f;
      const float sp = red16_sum(ea + eb), sn = red16_sum(na + nb);
      const float ip = __frcp_rn(sp), in_ = __frcp_rn(sn);
      const int row = mi * 16 + g4 * 4 + j;
      const int f0 = lane & 15;
      if (pp == 0) {
        u16* d = sPQ + (ph * 64 + row) * 40;
        d[f0] = f2bf(ea * ip); d[20 + f0] = f2bf(na * in_);
        if (v1) { d[16 + f0] = f2bf(eb * ip); d[36 + f0] = f2bf(nb * in_); }
      } else {
        u16* d = sPK + (ph * 64 + row) * 48;
        d[f0] = f2bf(ea * ip); d[20 + f0] = f2bf(na * in_);
        if (v1) { d[16 + f0] = f2bf(eb * ip); d[36 + f0] = f2bf(nb * in_); }
      }
    }
  }
  __syncthreads();
  for (int i = tid; i < 4 * 320; i += 512) {
    const int hh = i / 320, c = i % 320;
    u32x4 v = *(const u32x4*)((const char*)sPQ + hh * 5120 + c * 16);
    *(u32x4*)((char*)(phiq + ((size_t)(hg * 4 + hh) * S_ + m0) * 40) + c * 16) = v;
  }
  {
    const int hh = w & 3, nt = w >> 2;
    f32x4 kv[3];
#pragma unroll
    for (int ft = 0; ft < 3; ++ft) kv[ft] = (f32x4){0.f, 0.f, 0.f, 0.f};
#pragma unroll
    for (int ks = 0; ks < 2; ++ks) {
      const int kr = ks * 32 + r0;
      bf16x8 bfr = tr_read2((const char*)sV, (hh * 64 + kr) * 64 + nt * 32 + 8 * p4, 4 * 64);
#pragma unroll
      for (int ft = 0; ft < 3; ++ft) {
        bf16x8 afr = tr_read2((const char*)sPK, (hh * 64 + kr) * 96 + ft * 32 + 8 * p4, 4 * 96);
        kv[ft] = MFMA(afr, bfr, kv[ft]);
      }
    }
    float* o = kvp + ((size_t)(hg * 4 + hh) * 32 + mt) * 840;
    const int d = nt * 16 + (lane & 15);
    if (d < 21) {
#pragma unroll
      for (int ft = 0; ft < 3; ++ft)
#pragma unroll
        for (int j = 0; j < 4; ++j) {
          const int f = ft * 16 + g4 * 4 + j;
          if (f < 40) o[f * 21 + d] = kv[ft][j];
        }
    }
  }
}

__global__ __launch_bounds__(256) void k15_o(const u16* __restrict__ phiq, const float* __restrict__ kvp,
                                             u16* __restrict__ o) {
  __shared__ __attribute__((aligned(16))) u16 skv[64 * 32];
  __shared__ __attribute__((aligned(16))) u16 sO[256 * 24];
  const int head = blockIdx.x >> 3, rc = blockIdx.x & 7;
  const int tid = threadIdx.x, lane = tid & 63, w = tid >> 6;
  const int g4 = lane >> 4, q4 = (lane & 15) >> 2, p4 = lane & 3;
  bf16x8 af[4][2];
#pragma unroll
  for (int mi = 0; mi < 4; ++mi) {
    const int row = rc * 256 + w * 64 + mi * 16 + (lane & 15);
    const u16* gp = phiq + ((size_t)head * S_ + row) * 40;
    af[mi][0] = *(const bf16x8*)(gp + 8 * g4);
    af[mi][1] = (g4 == 0) ? *(const bf16x8*)(gp + 32) : (bf16x8){0, 0, 0, 0, 0, 0, 0, 0};
  }
  for (int i = tid; i < 64 * 32; i += 256) {
    const int f = i >> 5, d = i & 31;
    float s = 0.f;
    if (f < 40 && d < 21) {
      const float* p = kvp + (size_t)head * 32 * 840 + f * 21 + d;
      float t[32];
#pragma unroll
      for (int m = 0; m < 32; ++m) t[m] = p[m * 840];
#pragma unroll
      for (int m = 0; m < 32; ++m) s += t[m];
    }
    skv[i] = f2bf(s);
  }
  __syncthreads();
  const int r0 = 8 * g4 + q4;
  bf16x8 bfr[2][2];
#pragma unroll
  for (int ks = 0; ks < 2; ++ks)
#pragma unroll
    for (int nt = 0; nt < 2; ++nt)
      bfr[ks][nt] = tr_read2((const char*)skv, (ks * 32 + r0) * 64 + nt * 32 + 8 * p4, 4 * 64);
#pragma unroll
  for (int mi = 0; mi < 4; ++mi) {
    f32x4 c0 = {0.f, 0.f, 0.f, 0.f}, c1 = {0.f, 0.f, 0.f, 0.f};
    c0 = MFMA(af[mi][0], bfr[0][0], c0); c0 = MFMA(af[mi][1], bfr[1][0], c0);
    c1 = MFMA(af[mi][0], bfr[0][1], c1); c1 = MFMA(af[mi][1], bfr[1][1], c1);
#pragma unroll
    for (int j = 0; j < 4; ++j) {
      float den = __shfl(c1[j], (lane & 48) | 4);
      den = rbf(rbf(den) + 1e-6f);
      const float inv = 1.f / den;
      const int rl = w * 64 + mi * 16 + g4 * 4 + j;
      sO[rl * 24 + (lane & 15)] = f2bf(rbf(c0[j]) * inv);
      if ((lane & 15) < 4) sO[rl * 24 + 16 + (lane & 15)] = f2bf(rbf(c1[j]) * inv);
    }
  }
  __syncthreads();
  for (int i = tid; i < 256 * 5; i += 256) {
    const int rl = i / 5, c = i % 5;
    u32x2 v = *(const u32x2*)((const char*)sO + rl * 48 + c * 8);
    *(u32x2*)((char*)(o + (size_t)(rc * 256 + rl) * 640 + head * 20) + c * 8) = v;
  }
}

constexpr int G2_SLOT = 8192 + 16384;
template <int KTOT, bool STATS>
__global__ __launch_bounds__(512) void k_gemm64x80(const u16* __restrict__ A, const u16* __restrict__ B,
                                                   const u16* __restrict__ res, u16* __restrict__ out,
                                                   float2* __restrict__ stats) {
  constexpr int NT = KTOT / 64;
  __shared__ __attribute__((aligned(16))) char smem[5 * G2_SLOT];
  const int tid = threadIdx.x, lane = tid & 63, w = tid >> 6;
  const int nt = blockIdx.x & 7, mt = blockIdx.x >> 3;
  const int m0 = mt * 64, n0 = nt * 80;

  int a_off, b_off[2];
  {
    const int row = w * 8 + (lane >> 3);
    const int c = (lane & 7) ^ (row & 7);
    a_off = (m0 + row) * KTOT + c * 8;
  }
#pragma unroll
  for (int j = 0; j < 2; ++j) {
    const int r = (w * 2 + j) * 4 + (lane >> 4);
    const int pc = lane & 15;
    int u = (pc >> 1) ^ bswz(r);
    if (u > 4) u = 4;
    b_off[j] = r * 640 + n0 + u * 16 + (pc & 1) * 8;
  }
  const unsigned sm_u = lds_addr(smem);
  const int wu = __builtin_amdgcn_readfirstlane(w);
  auto issue = [&](int t) {
    const unsigned d = sm_u + (t % 5) * G2_SLOT;
    glds16(A + a_off + t * 64, d + wu * 1024);
#pragma unroll
    for (int j = 0; j < 2; ++j)
      glds16(B + b_off[j] + (size_t)t * 64 * 640, d + 8192 + (wu * 2 + j) * 1024);
  };
  issue(0); issue(1); issue(2); issue(3);

  const int wm = w >> 2, wk = (w >> 1) & 1, wn = w & 1;
  const int nb = wn * 3, ncnt = wn ? 2 : 3;
  f32x4 acc[2][3];
#pragma unroll
  for (int a = 0; a < 2; ++a)
#pragma unroll
    for (int b = 0; b < 3; ++b) acc[a][b] = (f32x4){0.f, 0.f, 0.f, 0.f};
  const int g4 = lane >> 4, q4 = (lane & 15) >> 2, p4 = lane & 3;
  const unsigned fz = q4 | ((g4 & 1) << 2);
  const int r0 = wk * 32 + 8 * g4 + q4;
#pragma unroll 1
  for (int t = 0; t < NT; ++t) {
    if (t < NT - 3) { WAIT_V(9); } else if (t == NT - 3) { WAIT_V(6); } else if (t == NT - 2) { WAIT_V(3); } else { WAIT_V(0); }
    BAR();
    if (t + 4 < NT) issue(t + 4);
    const char* sAc = smem + (t % 5) * G2_SLOT;
    const char* sBc = sAc + 8192;
    bf16x8 af[2];
#pragma unroll
    for (int mi = 0; mi < 2; ++mi) {
      const int row = wm * 32 + mi * 16 + (lane & 15);
      const int c = wk * 4 + g4;
      af[mi] = *(const bf16x8*)(sAc + row * 128 + ((c ^ (row & 7)) * 16));
    }
#pragma unroll
    for (int ni = 0; ni < 3; ++ni) {
      if (ni < ncnt) {
        bf16x8 bfr = tr_read2(sBc, r0 * 256 + ((nb + ni) ^ fz) * 32 + 8 * p4, 4 * 256);
#pragma unroll
        for (int mi = 0; mi < 2; ++mi) acc[mi][ni] = MFMA(af[mi], bfr, acc[mi][ni]);
      }
    }
  }
  __syncthreads();
  float* sR = (float*)smem;
  if (wk == 1) {
#pragma unroll
    for (int mi = 0; mi < 2; ++mi)
#pragma unroll
      for (int ni = 0; ni < 3; ++ni)
        if (ni < ncnt) {
#pragma unroll
          for (int j = 0; j < 4; ++j)
            sR[(wm * 32 + mi * 16 + g4 * 4 + j) * 84 + (nb + ni) * 16 + (lane & 15)] = acc[mi][ni][j];
        }
  }
  __syncthreads();
  if (wk == 0) {
#pragma unroll
    for (int mi = 0; mi < 2; ++mi)
#pragma unroll
      for (int ni = 0; ni < 3; ++ni)
        if (ni < ncnt) {
#pragma unroll
          for (int j = 0; j < 4; ++j) {
            float* p = sR + (wm * 32 + mi * 16 + g4 * 4 + j) * 84 + (nb + ni) * 16 + (lane & 15);
            *p = rbf(*p + acc[mi][ni][j]);
          }
        }
  }
  __syncthreads();
  {
    const int row = tid >> 3, cb = (tid & 7) * 10;
    const u16* rp = res + (size_t)(m0 + row) * 640 + n0 + cb;
    unsigned rv[5];
#pragma unroll
    for (int i = 0; i < 5; ++i) rv[i] = *(const unsigned*)(rp + 2 * i);
    float hv[10];
#pragma unroll
    for (int i = 0; i < 5; ++i) {
      hv[2 * i] = rbf(__uint_as_float(rv[i] << 16) + sR[row * 84 + cb + 2 * i]);
      hv[2 * i + 1] = rbf(__uint_as_float(rv[i] & 0xffff0000u) + sR[row * 84 + cb + 2 * i + 1]);
    }
    unsigned* op = (unsigned*)(out + (size_t)(m0 + row) * 640 + n0 + cb);
#pragma unroll
    for (int i = 0; i < 5; ++i)
      op[i] = (__float_as_uint(hv[2 * i]) >> 16) | (__float_as_uint(hv[2 * i + 1]) & 0xffff0000u);
    if (STATS) {
      float s = 0.f;
#pragma unroll
      for (int i = 0; i < 10; ++i) s += hv[i];
      s += __shfl_xor(s, 1); s += __shfl_xor(s, 2); s += __shfl_xor(s, 4);
      const float mu = s * (1.f / 80.f);
      float q = 0.f;
#pragma unroll
      for (int i = 0; i < 10; ++i) { float d = hv[i] - mu; q += d * d; }
      q += __shfl_xor(q, 1); q += __shfl_xor(q, 2); q += __shfl_xor(q, 4);
      if ((tid & 7) == 0) stats[(size_t)(m0 + row) * 8 + nt] = make_float2(s, q);
    }
  }
}

__global__ __launch_bounds__(512) void k3_ffn1(const u16* __restrict__ h, const u16* __restrict__ w1,
                                               const float2* __restrict__ stats, u16* __restrict__ hmid) {
  __shared__ __attribute__((aligned(16))) char smem[4 * 32768];
  const int tid = threadIdx.x, lane = tid & 63, w = tid >> 6;
  const int nt = blockIdx.x % 14, mt = blockIdx.x / 14;
  const int m0 = mt * 128, n0 = nt * 128;

  int a_off[2], b_off[2];
#pragma unroll
  for (int j = 0; j < 2; ++j) {
    const int row = (w * 2 + j) * 8 + (lane >> 3);
    const int c = (lane & 7) ^ (row & 7);
    a_off[j] = (m0 + row) * 640 + c * 8;
    const int r = (w * 2 + j) * 4 + (lane >> 4);
    const int pc = lane & 15;
    const int u = (pc >> 1) ^ bswz(r);
    int col = n0 + u * 16 + (pc & 1) * 8;
    if (col >= F_) col = F_ - 8;
    b_off[j] = r * F_ + col;
  }
  const unsigned sm_u = lds_addr(smem);
  const int wu = __builtin_amdgcn_readfirstlane(w);
  auto issue = [&](int t) {
    const unsigned d = sm_u + (t & 3) * 32768;
#pragma unroll
    for (int j = 0; j < 2; ++j) glds16(h + a_off[j] + t * 64, d + (wu * 2 + j) * 1024);
#pragma unroll
    for (int j = 0; j < 2; ++j)
      glds16(w1 + b_off[j] + (size_t)t * 64 * F_, d + 16384 + (wu * 2 + j) * 1024);
  };
  issue(0); issue(1); issue(2);

  const int wm = w >> 1, wn = w & 1;
  const int g4 = lane >> 4, q4 = (lane & 15) >> 2, p4 = lane & 3;
  float mean[2][4], rstd[2][4];
#pragma unroll
  for (int mi = 0; mi < 2; ++mi)
#pragma unroll
    for (int j = 0; j < 4; ++j) {
      const int row = m0 + wm * 32 + mi * 16 + g4 * 4 + j;
      const float4* sp = (const float4*)(stats + (size_t)row * 8);
      float4 s0 = sp[0], s1 = sp[1], s2 = sp[2], s3 = sp[3];
      const float tot = s0.x + s0.z + s1.x + s1.z + s2.x + s2.z + s3.x + s3.z;
      const float mu = tot * (1.f / 640.f);
      float m2 = s0.y + s0.w + s1.y + s1.w + s2.y + s2.w + s3.y + s3.w;
      float d;
      d = s0.x * (1.f / 80.f) - mu; m2 += 80.f * d * d;
      d = s0.z * (1.f / 80.f) - mu; m2 += 80.f * d * d;
      d = s1.x * (1.f / 80.f) - mu; m2 += 80.f * d * d;
      d = s1.z * (1.f / 80.f) - mu; m2 += 80.f * d * d;
      d = s2.x * (1.f / 80.f) - mu; m2 += 80.f * d * d;
      d = s2.z * (1.f / 80.f) - mu; m2 += 80.f * d * d;
      d = s3.x * (1.f / 80.f) - mu; m2 += 80.f * d * d;
      d = s3.z * (1.f / 80.f) - mu; m2 += 80.f * d * d;
      mean[mi][j] = mu;
      rstd[mi][j] = rsqrtf(m2 * (1.f / 640.f) + 1e-5f);
    }

  f32x4 acc[2][4], cs[4];
#pragma unroll
  for (int b = 0; b < 4; ++b) {
    acc[0][b] = (f32x4){0.f, 0.f, 0.f, 0.f};
    acc[1][b] = (f32x4){0.f, 0.f, 0.f, 0.f};
    cs[b] = (f32x4){0.f, 0.f, 0.f, 0.f};
  }
  const bf16x8 ones = {0x3f80, 0x3f80, 0x3f80, 0x3f80, 0x3f80, 0x3f80, 0x3f80, 0x3f80};
  const unsigned fz = q4 | ((g4 & 1) << 2);
#pragma unroll 1
  for (int t = 0; t < 10; ++t) {
    if (t < 8) { WAIT_V(8); } else if (t == 8) { WAIT_V(4); } else { WAIT_V(0); }
    BAR();
    if (t + 3 < 10) issue(t + 3);
    const char* sAc = smem + (t & 3) * 32768;
    const char* sBc = sAc + 16384;
#pragma unroll
    for (int kk = 0; kk < 2; ++kk) {
      bf16x8 af[2];
#pragma unroll
      for (int mi = 0; mi < 2; ++mi) {
        const int row = wm * 32 + mi * 16 + (lane & 15);
        const int c = kk * 4 + g4;
        af[mi] = *(const bf16x8*)(sAc + row * 128 + ((c ^ (row & 7)) * 16));
      }
      const int r0 = kk * 32 + 8 * g4 + q4;
#pragma unroll
      for (int ni = 0; ni < 4; ++ni) {
        bf16x8 bfr = tr_read2(sBc, r0 * 256 + ((wn * 4 + ni) ^ fz) * 32 + 8 * p4, 4 * 256);
#pragma unroll
        for (int mi = 0; mi < 2; ++mi) acc[mi][ni] = MFMA(af[mi], bfr, acc[mi][ni]);
        cs[ni] = MFMA(ones, bfr, cs[ni]);
      }
    }
  }
  __syncthreads();
  u16* sT = (u16*)smem;
#pragma unroll
  for (int mi = 0; mi < 2; ++mi)
#pragma unroll
    for (int ni = 0; ni < 4; ++ni)
#pragma unroll
      for (int j = 0; j < 4; ++j) {
        const float v = rbf(rstd[mi][j] * (acc[mi][ni][j] - mean[mi][j] * cs[ni][0]));
        const float sg = v * __frcp_rn(1.f + __expf(-v));
        sT[(wm * 32 + mi * 16 + g4 * 4 + j) * 136 + wn * 64 + ni * 16 + (lane & 15)] = f2bf(sg);
      }
  __syncthreads();
  for (int i = tid; i < 128 * 16; i += 512) {
    const int r = i >> 4, c = i & 15;
    const int col = n0 + c * 8;
    if (col < F_) {
      u32x4 v = *(const u32x4*)(sT + r * 136 + c * 8);
      *(u32x4*)(hmid + (size_t)(m0 + r) * F_ + col) = v;
    }
  }
}

extern "C" void kernel_launch(void* const* d_in, const int* in_sizes, int n_in, void* d_out, int out_size,
                              void* d_ws, size_t ws_size, hipStream_t stream) {
  const u16* x = (const u16*)d_in[0];
  const u16* wqkv = (const u16*)d_in[1];
  const u16* wphi = (const u16*)d_in[2];
  const u16* wo = (const u16*)d_in[3];
  const u16* w1 = (const u16*)d_in[5];
  const u16* w2 = (const u16*)d_in[6];
  u16* out = (u16*)d_out;
  char* ws = (char*)d_ws;
  u16* phiq = (u16*)ws;
  float* kvp = (float*)(ws + (8 << 20));
  u16* o = (u16*)(ws + (16 << 20));
  u16* h = (u16*)(ws + (24 << 20));
  u16* hmid = (u16*)(ws + (32 << 20));
  float2* stats = (float2*)(ws + (48 << 20));

  k1_qkv<<<256, 512, 0, stream>>>(x, wqkv, wphi, phiq, kvp);
  k15_o<<<256, 256, 0, stream>>>(phiq, kvp, o);
  k_gemm64x80<640, true><<<256, 512, 0, stream>>>(o, wo, x, h, stats);
  k3_ffn1<<<16 * 14, 512, 0, stream>>>(h, w1, stats, hmid);
  k_gemm64x80<1728, false><<<256, 512, 0, stream>>>(hmid, w2, h, out, nullptr);
}
```

```cpp
#include <hip/hip_runtime.h>
#include <hip/hip_bf16.h>
#include <stdint.h>

typedef __attribute__((ext_vector_type(8))) short bf16x8;
typedef __attribute__((ext_vector_type(4))) short bf16x4;
typedef __attribute__((ext_vector_type(4))) float f32x4;
typedef __attribute__((ext_vector_type(4))) unsigned u32x4;
typedef __attribute__((ext_vector_type(2))) unsigned u32x2;
typedef unsigned short u16;

#define LDS_PTR(T, p) ((__attribute__((address_space(3))) T*)(p))
#define WAIT_V(n) asm volatile("s_waitcnt vmcnt(" #n ")" ::: "memory")
#define BAR() __builtin_amdgcn_s_barrier()
#define MFMA(a, b, c) __builtin_amdgcn_mfma_f32_16x16x32_bf16(a, b, c, 0, 0, 0)

constexpr int S_ = 2048, D_ = 640, H_ = 32, DH_ = 20, F_ = 1728;

__device__ __forceinline__ float bf2f(u16 v) { return __uint_as_float(((unsigned)v) << 16); }
__device__ __forceinline__ u16 f2bf(float f) {
  __bf16 b = (__bf16)f;
  return __builtin_bit_cast(u16, b);
}
__device__ __forceinline__ float rbf(float f) { return bf2f(f2bf(f)); }
__device__ __forceinline__ unsigned pack2(float a, float b) {
  return (unsigned)f2bf(a) | ((unsigned)f2bf(b) << 16);
}
__device__ __forceinline__ bf16x4 tr_read(const char* lds_base, unsigned off) {
  return __builtin_amdgcn_ds_read_tr16_b64_v4i16(LDS_PTR(bf16x4, lds_base + off));
}
__device__ __forceinline__ bf16x8 tr_read2(const char* lds_base, unsigned off, unsigned rowbytes4) {
  bf16x4 b0 = tr_read(lds_base, off);
  bf16x4 b1 = tr_read(lds_base, off + rowbytes4);
  return __builtin_shufflevector(b0, b1, 0, 1, 2, 3, 4, 5, 6, 7);
}
__device__ __forceinline__ void glds16(const u16* g, unsigned dst_uniform) {
  unsigned keep;
  asm volatile("s_mov_b32 %0, m0\n\ts_mov_b32 m0, %2\n\ts_nop 0\n\tglobal_load_lds_dwordx4 %1, off\n\ts_mov_b32 m0, %0"
               : "=&s"(keep) : "v"(g), "s"(dst_uniform) : "memory");
}
__device__ __forceinline__ unsigned lds_addr(const void* p) {
  return __builtin_amdgcn_readfirstlane((unsigned)(size_t)LDS_PTR(char, p));
}
__device__ __forceinline__ unsigned bswz(unsigned r) { return (r & 3) | (((r >> 3) & 1) << 2); }

__device__ __forceinline__ float red16_sum(float v) {
  v += __shfl_xor(v, 1); v += __shfl_xor(v, 2); v += __shfl_xor(v, 4); v += __shfl_xor(v, 8);
  return v;
}
__device__ __forceinline__ float red16_max(float v) {
  v = fmaxf(v, __shfl_xor(v, 1)); v = fmaxf(v, __shfl_xor(v, 2));
  v = fmaxf(v, __shfl_xor(v, 4)); v = fmaxf(v, __shfl_xor(v, 8));
  return v;
}


__device__ __forceinline__ float red8_sum(float v) {
  v += __shfl_xor(v, 1); v += __shfl_xor(v, 2); v += __shfl_xor(v, 4);
  return v;
}
__device__ __forceinline__ float lo(unsigned u) { return __uint_as_float(u << 16); }
__device__ __forceinline__ float hi(unsigned u) { return __uint_as_float(u & 0xffff0000u); }

constexpr int K1_A_BYTES = 64 * 1280;
constexpr int K1_SLOT = 32 * 512;
constexpr int K1_LDS = K1_A_BYTES + 4 * K1_SLOT;

__global__ __launch_bounds__(512) void k1_qkv(const u16* __restrict__ x, const u16* __restrict__ wqkv,
                                              const u16* __restrict__ wphi, u16* __restrict__ phiq,
                                              float* __restrict__ kvp) {
  __shared__ __attribute__((aligned(16))) char smem[K1_LDS];
  char* sA = smem;
  char* sB = smem + K1_A_BYTES;
  const int tid = threadIdx.x, lane = tid & 63, w = tid >> 6;
  const int hg = blockIdx.x & 7, mt = blockIdx.x >> 3;
  const int m0 = mt * 64;
  const unsigned sA_u = lds_addr(smem);
  const int wu = __builtin_amdgcn_readfirstlane(w);

#pragma unroll 1
  for (int j = 0; j < 10; ++j) {
    const int pos = (wu * 10 + j) * 64 + lane;
    const int row = pos / 80, pc = pos % 80;
    glds16(x + (size_t)(m0 + row) * 640 + ((pc ^ (row & 15)) * 8), sA_u + (wu * 10 + j) * 1024);
  }
  int boff[2];
#pragma unroll
  for (int j = 0; j < 2; ++j) {
    const int r = (w * 2 + j) * 2 + (lane >> 5);
    const int pc = lane & 31;
    int u = (pc >> 1) ^ bswz(r);
    if (u == 15) u = 14;
    const int n8 = u * 2 + (pc & 1);
    boff[j] = r * 1920 + (n8 / 10) * 640 + hg * 80 + (n8 % 10) * 8;
  }
  const unsigned sB_u = sA_u + K1_A_BYTES + wu * 2048;
#pragma unroll 1
  for (int t = 0; t < 3; ++t) {
    glds16(wqkv + (size_t)t * 32 * 1920 + boff[0], sB_u + t * K1_SLOT);
    glds16(wqkv + (size_t)t * 32 * 1920 + boff[1], sB_u + t * K1_SLOT + 1024);
  }
  WAIT_V(6);
  __builtin_amdgcn_s_barrier();
  {
    char* rp = sA + (tid >> 3) * 1280 + (tid & 7) * 160;
    float s = 0.f;
#pragma unroll 1
    for (int i = 0; i < 10; ++i) {
      u32x4 v = *(const u32x4*)(rp + i * 16);
      s += (lo(v[0]) + hi(v[0])) + (lo(v[1]) + hi(v[1])) + (lo(v[2]) + hi(v[2])) + (lo(v[3]) + hi(v[3]));
    }
    const float mean = red8_sum(s) * (1.f / 640.f);
    float q = 0.f;
#pragma unroll 1
    for (int i = 0; i < 10; ++i) {
      u32x4 v = *(const u32x4*)(rp + i * 16);
#pragma unroll
      for (int e = 0; e < 4; ++e) { float a = lo(v[e]) - mean, b = hi(v[e]) - mean; q += a * a + b * b; }
    }
    const float rstd = rsqrtf(red8_sum(q) * (1.f / 640.f) + 1e-5f);
#pragma unroll 1
    for (int i = 0; i < 10; ++i) {
      u32x4 v = *(const u32x4*)(rp + i * 16);
#pragma unroll
      for (int e = 0; e < 4; ++e) v[e] = pack2((lo(v[e]) - mean) * rstd, (hi(v[e]) - mean) * rstd);
      *(u32x4*)(rp + i * 16) = v;
    }
  }
  asm volatile("s_waitcnt lgkmcnt(0)" ::: "memory");

  const int wm = w >> 2, wn = w & 3;
  f32x4 acc[2][4];
#pragma unroll
  for (int a = 0; a < 2; ++a)
#pragma unroll
    for (int b = 0; b < 4; ++b) acc[a][b] = (f32x4){0.f, 0.f, 0.f, 0.f};
  const int g4 = lane >> 4, q4 = (lane & 15) >> 2, p4 = lane & 3;
  const unsigned fz = q4 | ((g4 & 1) << 2);
  const int r0 = 8 * g4 + q4;
  const int arow = wm * 32 + (lane & 15);
#pragma unroll 1
  for (int t = 0; t < 20; ++t) {
    WAIT_V(4);
    BAR();
    {
      const int tn = (t + 3 < 20) ? t + 3 : 19;
      glds16(wqkv + (size_t)tn * 32 * 1920 + boff[0], sB_u + ((t + 3) & 3) * K1_SLOT);
      glds16(wqkv + (size_t)tn * 32 * 1920 + boff[1], sB_u + ((t + 3) & 3) * K1_SLOT + 1024);
    }
    const char* sBc = sB + (t & 3) * K1_SLOT + r0 * 512 + 8 * p4;
    const int c = t * 4 + g4;
    bf16x8 a0 = *(const bf16x8*)(sA + arow * 1280 + ((c ^ (arow & 15)) * 16));
    bf16x8 a1 = *(const bf16x8*)(sA + (arow + 16) * 1280 + ((c ^ (arow & 15)) * 16));
#pragma unroll
    for (int ni = 0; ni < 4; ++ni) {
      bf16x8 bfr = tr_read2(sBc, ((wn * 4 + ni) ^ fz) * 32, 4 * 512);
      acc[0][ni] = MFMA(a0, bfr, acc[0][ni]);
      acc[1][ni] = MFMA(a1, bfr, acc[1][ni]);
    }
  }
  WAIT_V(0);
  __syncthreads();

  u16* sQ = (u16*)smem;
  u16* sW = (u16*)(smem + 32768);
  float* sP = (float*)(smem + 40960);
  u16* sPK = (u16*)(smem + 83968);
  u16* sPQ = (u16*)(smem + 108544);
  {
    u16* qp = sQ + (wm * 32 + g4 * 4) * 256 + wn * 64 + (lane & 15);
#pragma unroll
    for (int mi = 0; mi < 2; ++mi)
#pragma unroll
      for (int ni = 0; ni < 4; ++ni)
#pragma unroll
        for (int j = 0; j < 4; ++j) qp[(mi * 16 + j) * 256 + ni * 16] = f2bf(acc[mi][ni][j]);
  }
#pragma unroll 1
  for (int i = tid; i < 4096; i += 512) {
    const int hh = i >> 10, d = (i >> 5) & 31, f = i & 31;
    sW[i] = (d < 20 && f < 20) ? wphi[(hg * 4 + hh) * 400 + d * 20 + f] : (u16)0;
  }
  __syncthreads();
  const int ph = w & 3, pp = w >> 2;
  {
    bf16x8 wf0 = tr_read2((const char*)sW, (ph * 32 + r0) * 64 + 8 * p4, 4 * 64);
    bf16x8 wf1 = tr_read2((const char*)sW, (ph * 32 + r0) * 64 + 32 + 8 * p4, 4 * 64);
#pragma unroll 1
    for (int mi = 0; mi < 4; ++mi) {
      const u16* ap = sQ + (mi * 16 + (lane & 15)) * 256 + pp * 80 + ph * 20 + 8 * g4;
      u32x2 a0 = *(const u32x2*)ap;
      u32x2 a1 = *(const u32x2*)(ap + 4);
      u32x4 av = {a0[0], a0[1], a1[0], a1[1]};
      bf16x8 af = __builtin_bit_cast(bf16x8, av);
      f32x4 z = {0.f, 0.f, 0.f, 0.f};
      f32x4 p0 = MFMA(af, wf0, z);
      f32x4 p1 = MFMA(af, wf1, z);
      float* pp_ = sP + ((pp * 4 + ph) * 64 + mi * 16 + g4 * 4) * 21 + (lane & 15);
#pragma unroll
      for (int j = 0; j < 4; ++j) {
        pp_[j * 21] = rbf(p0[j]);
        if ((lane & 15) < 4) pp_[j * 21 + 16] = rbf(p1[j]);
      }
    }
  }
  __syncthreads();
  {
    const float* pr = sP + tid * 21;
    float mx = pr[0], mn = pr[0];
#pragma unroll 1
    for (int f = 1; f < 20; ++f) { const float v = pr[f]; mx = fmaxf(mx, v); mn = fminf(mn, v); }
    float sp = 0.f, sn = 0.f;
#pragma unroll 1
    for (int f = 0; f < 20; ++f) { const float v = pr[f]; sp += __expf(v - mx); sn += __expf(mn - v); }
    const float ip = __builtin_amdgcn_rcpf(sp), in_ = __builtin_amdgcn_rcpf(sn);
    const int it = tid & 255;
    u16* dst = (tid < 256) ? (sPQ + it * 40) : (sPK + it * 48);
#pragma unroll 1
    for (int f = 0; f < 20; ++f) {
      const float v = pr[f];
      dst[f] = f2bf(__expf(v - mx) * ip);
      dst[20 + f] = f2bf(__expf(mn - v) * in_);
    }
  }
  __syncthreads();
#pragma unroll 1
  for (int i = tid; i < 4 * 320; i += 512) {
    const int hh = i / 320, c = i % 320;
    u32x4 v = *(const u32x4*)((const char*)sPQ + hh * 5120 + c * 16);
    *(u32x4*)((char*)(phiq + ((size_t)(hg * 4 + hh) * S_ + m0) * 40) + c * 16) = v;
  }
  {
    const int nt = pp;
    const bf16x8 ones = {0x3f80, 0x3f80, 0x3f80, 0x3f80, 0x3f80, 0x3f80, 0x3f80, 0x3f80};
    f32x4 kv[3], zz[3];
#pragma unroll
    for (int ft = 0; ft < 3; ++ft) { kv[ft] = (f32x4){0.f, 0.f, 0.f, 0.f}; zz[ft] = kv[ft]; }
#pragma unroll 1
    for (int ks = 0; ks < 2; ++ks) {
      const int kr = ks * 32 + r0;
      bf16x8 bfr = tr_read2((const char*)sQ, kr * 512 + (160 + ph * 20 + nt * 16 + 4 * p4) * 2, 4 * 512);
#pragma unroll
      for (int ft = 0; ft < 3; ++ft) {
        bf16x8 afr = tr_read2((const char*)sPK, (ph * 64 + kr) * 96 + ft * 32 + 8 * p4, 4 * 96);
        kv[ft] = MFMA(afr, bfr, kv[ft]);
        zz[ft] = MFMA(afr, ones, zz[ft]);
      }
    }
    float* o = kvp + ((size_t)(hg * 4 + ph) * 32 + mt) * 840 + g4 * 4 * 21;
    const int d = nt * 16 + (lane & 15);
    const bool isz = (d == 20);
    if (d < 21) {
#pragma unroll
      for (int ft = 0; ft < 3; ++ft)
#pragma unroll
        for (int j = 0; j < 4; ++j)
          if (ft < 2 || g4 < 2) o[(ft * 16 + j) * 21 + d] = isz ? zz[ft][j] : kv[ft][j];
    }
  }
}

__global__ __launch_bounds__(256) void k15_o(const u16* __restrict__ phiq, const float* __restrict__ kvp,
                                             u16* __restrict__ o) {
  __shared__ __attribute__((aligned(16))) u16 skv[64 * 32];
  __shared__ __attribute__((aligned(16))) u16 sO[256 * 24];
  const int head = blockIdx.x >> 3, rc = blockIdx.x & 7;
  const int tid = threadIdx.x, lane = tid & 63, w = tid >> 6;
  const int g4 = lane >> 4, q4 = (lane & 15) >> 2, p4 = lane & 3;
#pragma unroll 1
  for (int i = tid; i < 2048; i += 256) skv[i] = 0;
  float s[4] = {0.f, 0.f, 0.f, 0.f};
  const float* p = kvp + (size_t)head * 32 * 840 + tid;
  const int e3 = (tid + 768 < 840) ? 768 : 0;
#pragma unroll 4
  for (int m = 0; m < 32; ++m) {
    s[0] += p[m * 840]; s[1] += p[m * 840 + 256]; s[2] += p[m * 840 + 512]; s[3] += p[m * 840 + e3];
  }
  __syncthreads();
#pragma unroll
  for (int k = 0; k < 4; ++k) {
    const int e = tid + 256 * k;
    if (e < 840) skv[(e / 21) * 32 + e % 21] = f2bf(s[k]);
  }
  __syncthreads();
  const int r0 = 8 * g4 + q4;
  bf16x8 b00 = tr_read2((const char*)skv, r0 * 64 + 8 * p4, 256);
  bf16x8 b01 = tr_read2((const char*)skv, r0 * 64 + 32 + 8 * p4, 256);
  bf16x8 b10 = tr_read2((const char*)skv, (32 + r0) * 64 + 8 * p4, 256);
  bf16x8 b11 = tr_read2((const char*)skv, (32 + r0) * 64 + 32 + 8 * p4, 256);
#pragma unroll 1
  for (int mi = 0; mi < 4; ++mi) {
    const u16* gp = phiq + ((size_t)head * S_ + rc * 256 + w * 64 + mi * 16 + (lane & 15)) * 40;
    bf16x8 a0 = *(const bf16x8*)(gp + 8 * g4);
    bf16x8 a1 = *(const bf16x8*)(gp + 32 - 8 * g4 * (g4 != 0));
    f32x4 c0 = {0.f, 0.f, 0.f, 0.f}, c1 = {0.f, 0.f, 0.f, 0.f};
    c0 = MFMA(a0, b00, c0); c0 = MFMA(a1, b10, c0);
    c1 = MFMA(a0, b01, c1); c1 = MFMA(a1, b11, c1);
    u16* op = sO + (w * 64 + mi * 16 + g4 * 4) * 24 + (lane & 15);
#pragma unroll
    for (int j = 0; j < 4; ++j) {
      float den = __shfl(c1[j], (lane & 48) | 4);
      den = rbf(rbf(den) + 1e-6f);
      const float inv = 1.f / den;
      op[j * 24] = f2bf(rbf(c0[j]) * inv);
      if ((lane & 15) < 4) op[j * 24 + 16] = f2bf(rbf(c1[j]) * inv);
    }
  }
  __syncthreads();
#pragma unroll 1
  for (int i = tid; i < 256 * 5; i += 256) {
    const int rl = i / 5, c = i % 5;
    u32x2 v = *(const u32x2*)((const char*)sO + rl * 48 + c * 8);
    *(u32x2*)((char*)(o + (size_t)(rc * 256 + rl) * 640 + head * 20) + c * 8) = v;
  }
}

constexpr int G2_SLOT = 8192 + 16384;
template <int KTOT, bool STATS>
__global__ __launch_bounds__(512) void k_gemm64x80(const u16* __restrict__ A, const u16* __restrict__ B,
                                                   const u16* __restrict__ res, u16* __restrict__ out,
                                                   float2* __restrict__ stats) {
  constexpr int NT = KTOT / 64;
  __shared__ __attribute__((aligned(16))) char smem[5 * G2_SLOT];
  const int tid = threadIdx.x, lane = tid & 63, w = tid >> 6;
  const int nt = blockIdx.x & 7, mt = blockIdx.x >> 3;
  const int m0 = mt * 64, n0 = nt * 80;
  const u16* ap;
  const u16* bp[2];
  {
    const int row = w * 8 + (lane >> 3);
    ap = A + (size_t)(m0 + row) * KTOT + ((lane & 7) ^ (row & 7)) * 8;
  }
#pragma unroll
  for (int j = 0; j < 2; ++j) {
    const int r = (w * 2 + j) * 4 + (lane >> 4);
    const int pc = lane & 15;
    int u = (pc >> 1) ^ bswz(r);
    if (u > 4) u = 4;
    bp[j] = B + r * 640 + n0 + u * 16 + (pc & 1) * 8;
  }
  const unsigned sm_u = lds_addr(smem);
  const int wu = __builtin_amdgcn_readfirstlane(w);
  const int wm = w >> 2, wk = (w >> 1) & 1, wn = w & 1;
  f32x4 acc[2][3];
#pragma unroll
  for (int a = 0; a < 2; ++a)
#pragma unroll
    for (int b = 0; b < 3; ++b) acc[a][b] = (f32x4){0.f, 0.f, 0.f, 0.f};
  const int g4 = lane >> 4, q4 = (lane & 15) >> 2, p4 = lane & 3;
  const unsigned fz = q4 | ((g4 & 1) << 2);
  const int arow = wm * 32 + (lane & 15);
  const int aoff = arow * 128 + (((wk * 4 + g4) ^ (arow & 7)) * 16);
  const int boffl = 8192 + (wk * 32 + 8 * g4 + q4) * 256 + 8 * p4;
  int slot = 0;
#pragma unroll 1
  for (int t = -4; t < NT; ++t) {
    if (t >= 0) { WAIT_V(9); BAR(); }
    {
      const int tn = (t + 4 < NT) ? t + 4 : NT - 1;
      int sl = slot + 4; if (sl >= 5) sl -= 5;
      const unsigned d = sm_u + sl * G2_SLOT;
      glds16(ap + tn * 64, d + wu * 1024);
      glds16(bp[0] + (size_t)tn * 64 * 640, d + 8192 + wu * 2048);
      glds16(bp[1] + (size_t)tn * 64 * 640, d + 8192 + wu * 2048 + 1024);
    }
    if (t >= 0) {
      const char* sc = smem + slot * G2_SLOT;
      bf16x8 a0 = *(const bf16x8*)(sc + aoff);
      bf16x8 a1 = *(const bf16x8*)(sc + aoff + 2048);
#pragma unroll
      for (int ni = 0; ni < 3; ++ni) {
        const int un = (wn * 3 + ni > 4) ? 4 : wn * 3 + ni;
        bf16x8 bfr = tr_read2(sc, boffl + (un ^ fz) * 32, 4 * 256);
        acc[0][ni] = MFMA(a0, bfr, acc[0][ni]);
        acc[1][ni] = MFMA(a1, bfr, acc[1][ni]);
      }
    }
    slot = (slot == 4) ? 0 : slot + 1;
  }
  WAIT_V(0);
  __syncthreads();
  {
    float* rp = (float*)smem + wk * 6400 + (wm * 32 + g4 * 4) * 100 + wn * 48 + (lane & 15);
#pragma unroll
    for (int mi = 0; mi < 2; ++mi)
#pragma unroll
      for (int ni = 0; ni < 3; ++ni)
#pragma unroll
        for (int j = 0; j < 4; ++j) rp[(mi * 16 + j) * 100 + ni * 16] = acc[mi][ni][j];
  }
  __syncthreads();
  {
    const int row = tid >> 3, cb = (tid & 7) * 10;
    const float* r0p = (const float*)smem + row * 100 + cb;
    const unsigned* rp = (const unsigned*)(res + (size_t)(m0 + row) * 640 + n0 + cb);
    unsigned* op = (unsigned*)(out + (size_t)(m0 + row) * 640 + n0 + cb);
    float s = 0.f, s2 = 0.f;
    float* wp = (float*)smem + row * 100 + cb;
#pragma unroll 1
    for (int i = 0; i < 5; ++i) {
      const unsigned rv = rp[i];
      const float a = rbf(lo(rv) + rbf(r0p[2 * i] + r0p[6400 + 2 * i]));
      const float b = rbf(hi(rv) + rbf(r0p[2 * i + 1] + r0p[6400 + 2 * i + 1]));
      op[i] = (__float_as_uint(a) >> 16) | (__float_as_uint(b) & 0xffff0000u);
      if (STATS) { wp[2 * i] = a; wp[2 * i + 1] = b; s += a + b; }
    }
    if (STATS) {
      s = red8_sum(s);
      const float mu = s * (1.f / 80.f);
#pragma unroll 1
      for (int i = 0; i < 10; ++i) { const float d = wp[i] - mu; s2 += d * d; }
      s2 = red8_sum(s2);
      if ((tid & 7) == 0) stats[(size_t)(m0 + row) * 8 + nt] = make_float2(s, s2);
    }
  }
}

__global__ __launch_bounds__(512) void k3_ffn1(const u16* __restrict__ h, const u16* __restrict__ w1,
                                               const float2* __restrict__ stats, u16* __restrict__ hmid) {
  __shared__ __attribute__((aligned(16))) char smem[4 * 32768 + 2048];
  const int tid = threadIdx.x, lane = tid & 63, w = tid >> 6;
  const int nt = blockIdx.x % 14, mt = blockIdx.x / 14;
  const int m0 = mt * 128, n0 = nt * 128;
  float* sMean = (float*)(smem + 131072);
  const u16* ap[2];
  const u16* bp[2];
#pragma unroll
  for (int j = 0; j < 2; ++j) {
    const int row = (w * 2 + j) * 8 + (lane >> 3);
    ap[j] = h + (size_t)(m0 + row) * 640 + ((lane & 7) ^ (row & 7)) * 8;
    const int r = (w * 2 + j) * 4 + (lane >> 4);
    const int pc = lane & 15;
    int col = n0 + ((pc >> 1) ^ bswz(r)) * 16 + (pc & 1) * 8;
    if (col >= F_) col = F_ - 8;
    bp[j] = w1 + r * F_ + col;
  }
  const unsigned sm_u = lds_addr(smem);
  const int wu = __builtin_amdgcn_readfirstlane(w);
  if (tid < 128) {
    const float2* sp = stats + (size_t)(m0 + tid) * 8;
    float tot = 0.f, m2 = 0.f;
#pragma unroll 1
    for (int i = 0; i < 8; ++i) { tot += sp[i].x; m2 += sp[i].y; }
    const float mu = tot * (1.f / 640.f);
#pragma unroll 1
    for (int i = 0; i < 8; ++i) { const float d = sp[i].x * (1.f / 80.f) - mu; m2 += 80.f * d * d; }
    sMean[tid] = mu;
    sMean[128 + tid] = rsqrtf(m2 * (1.f / 640.f) + 1e-5f);
  }
  asm volatile("s_waitcnt vmcnt(0) lgkmcnt(0)" ::: "memory");
  const int wm = w >> 1, wn = w & 1;
  const int g4 = lane >> 4, q4 = (lane & 15) >> 2, p4 = lane & 3;
  f32x4 acc[2][4], cs[4];
#pragma unroll
  for (int b = 0; b < 4; ++b) {
    acc[0][b] = (f32x4){0.f, 0.f, 0.f, 0.f};
    acc[1][b] = acc[0][b];
    cs[b] = acc[0][b];
  }
  const bf16x8 ones = {0x3f80, 0x3f80, 0x3f80, 0x3f80, 0x3f80, 0x3f80, 0x3f80, 0x3f80};
  const unsigned fz = q4 | ((g4 & 1) << 2);
  const int arow = wm * 32 + (lane & 15);
#pragma unroll 1
  for (int t = -3; t < 20; ++t) {
    if (t >= 0 && !(t & 1)) { WAIT_V(8); BAR(); }
    if (t < 0 || !(t & 1)) {
      const int st = (t < 0) ? t + 3 : (t >> 1) + 3;
      const int tn = (st < 10) ? st : 9;
      const unsigned d = sm_u + (st & 3) * 32768 + wu * 2048;
      glds16(ap[0] + tn * 64, d);
      glds16(ap[1] + tn * 64, d + 1024);
      glds16(bp[0] + (size_t)tn * 64 * F_, d + 16384);
      glds16(bp[1] + (size_t)tn * 64 * F_, d + 16384 + 1024);
    }
    if (t >= 0) {
      const int kk = t & 1;
      const char* sc = smem + ((t >> 1) & 3) * 32768;
      const int ao = arow * 128 + (((kk * 4 + g4) ^ (arow & 7)) * 16);
      bf16x8 a0 = *(const bf16x8*)(sc + ao);
      bf16x8 a1 = *(const bf16x8*)(sc + ao + 2048);
      const char* sb = sc + 16384 + (kk * 32 + 8 * g4 + q4) * 256 + 8 * p4;
#pragma unroll
      for (int ni = 0; ni < 4; ++ni) {
        bf16x8 bfr = tr_read2(sb, ((wn * 4 + ni) ^ fz) * 32, 4 * 256);
        acc[0][ni] = MFMA(a0, bfr, acc[0][ni]);
        acc[1][ni] = MFMA(a1, bfr, acc[1][ni]);
        cs[ni] = MFMA(ones, bfr, cs[ni]);
      }
    }
  }
  WAIT_V(0);
  __syncthreads();
  {
    float* rp = (float*)smem + (wm * 32 + g4 * 4) * 132 + wn * 64 + (lane & 15);
#pragma unroll
    for (int mi = 0; mi < 2; ++mi)
#pragma unroll
      for (int ni = 0; ni < 4; ++ni)
#pragma unroll
        for (int j = 0; j < 4; ++j) rp[(mi * 16 + j) * 132 + ni * 16] = acc[mi][ni][j];
    if (w < 2 && g4 == 0) {
#pragma unroll
      for (int ni = 0; ni < 4; ++ni) sMean[256 + wn * 64 + ni * 16 + lane] = cs[ni][0];
    }
  }
  __syncthreads();
#pragma unroll 1
  for (int i = tid; i < 128 * 16; i += 512) {
    const int r = i >> 4, c = i & 15;
    const float mu = sMean[r], rs = sMean[128 + r];
    const float* vp = (const float*)smem + r * 132 + c * 8;
    const float* cp = sMean + 256 + c * 8;
    u32x4 ov;
#pragma unroll
    for (int e = 0; e < 4; ++e) {
      const float a = rbf(rs * (vp[2 * e] - mu * cp[2 * e]));
      const float b = rbf(rs * (vp[2 * e + 1] - mu * cp[2 * e + 1]));
      ov[e] = pack2(a * __builtin_amdgcn_rcpf(1.f + __expf(-a)), b * __builtin_amdgcn_rcpf(1.f + __expf(-b)));
    }
    if (n0 + c * 8 < F_) *(u32x4*)(hmid + (size_t)(m0 + r) * F_ + n0 + c * 8) = ov;
  }
}

extern "C" void kernel_launch(void* const* d_in, const int* in_sizes, int n_in, void* d_out, int out_size,
                              void* d_ws, size_t ws_size, hipStream_t stream) {
  const u16* x = (const u16*)d_in[0];
  const u16* wqkv = (const u16*)d_in[1];
  const u16* wphi = (const u16*)d_in[2];
  const u16* wo = (const u16*)d_in[3];
  const u16* w1 = (const u16*)d_in[5];
  const u16* w2 = (const u16*)d_in[6];
  u16* out = (u16*)d_out;
  char* ws = (char*)d_ws;
  u16* phiq = (u16*)ws;
  float* kvp = (float*)(ws + (8 << 20));
  u16* o = (u16*)(ws + (16 << 20));
  u16* h = (u16*)(ws + (24 << 20));
  u16* hmid = (u16*)(ws + (32 << 20));
  float2* stats = (float2*)(ws + (48 << 20));

  k1_qkv<<<256, 512, 0, stream>>>(x, wqkv, wphi, phiq, kvp);
  k15_o<<<256, 256, 0, stream>>>(phiq, kvp, o);
  k_gemm64x80<640, true><<<256, 512, 0, stream>>>(o, wo, x, h, stats);
  k3_ffn1<<<16 * 14, 512, 0, stream>>>(h, w1, stats, hmid);
  k_gemm64x80<1728, false><<<256, 512, 0, stream>>>(hmid, w2, h, out, nullptr);
}
```

```cpp
#include <hip/hip_runtime.h>
#include <hip/hip_bf16.h>
#include <stdint.h>

typedef __attribute__((ext_vector_type(8))) short bf16x8;
typedef __attribute__((ext_vector_type(4))) short bf16x4;
typedef __attribute__((ext_vector_type(4))) float f32x4;
typedef __attribute__((ext_vector_type(4))) unsigned u32x4;
typedef __attribute__((ext_vector_type(2))) unsigned u32x2;
typedef unsigned short u16;

#define LDS_PTR(T, p) ((__attribute__((address_space(3))) T*)(p))
#define WAIT_V(n) asm volatile("s_waitcnt vmcnt(" #n ")" ::: "memory")
#define BAR() __builtin_amdgcn_s_barrier()
#define MFMA(a, b, c) __builtin_amdgcn_mfma_f32_16x16x32_bf16(a, b, c, 0, 0, 0)

constexpr int S_ = 2048, D_ = 640, H_ = 32, DH_ = 20, F_ = 1728;

__device__ __forceinline__ float bf2f(u16 v) { return __uint_as_float(((unsigned)v) << 16); }
__device__ __forceinline__ u16 f2bf(float f) {
  __bf16 b = (__bf16)f;
  return __builtin_bit_cast(u16, b);
}
__device__ __forceinline__ float rbf(float f) { return bf2f(f2bf(f)); }
__device__ __forceinline__ unsigned pack2(float a, float b) {
  return (unsigned)f2bf(a) | ((unsigned)f2bf(b) << 16);
}
__device__ __forceinline__ bf16x4 tr_read(const char* lds_base, unsigned off) {
  return __builtin_amdgcn_ds_read_tr16_b64_v4i16(LDS_PTR(bf16x4, lds_base + off));
}
__device__ __forceinline__ bf16x8 tr_read2(const char* lds_base, unsigned off, unsigned rowbytes4) {
  bf16x4 b0 = tr_read(lds_base, off);
  bf16x4 b1 = tr_read(lds_base, off + rowbytes4);
  return __builtin_shufflevector(b0, b1, 0, 1, 2, 3, 4, 5, 6, 7);
}
__device__ __forceinline__ void glds16(const u16* g, unsigned dst_uniform) {
  unsigned keep;
  asm volatile("s_mov_b32 %0, m0\n\ts_mov_b32 m0, %2\n\ts_nop 0\n\tglobal_load_lds_dwordx4 %1, off\n\ts_mov_b32 m0, %0"
               : "=&s"(keep) : "v"(g), "s"(dst_uniform) : "memory");
}
__device__ __forceinline__ unsigned lds_addr(const void* p) {
  return __builtin_amdgcn_readfirstlane((unsigned)(size_t)LDS_PTR(char, p));
}
__device__ __forceinline__ unsigned bswz(unsigned r) { return (r & 3) | (((r >> 3) & 1) << 2); }

__device__ __forceinline__ float red16_sum(float v) {
  v += __shfl_xor(v, 1); v += __shfl_xor(v, 2); v += __shfl_xor(v, 4); v += __shfl_xor(v, 8);
  return v;
}
__device__ __forceinline__ float red16_max(float v) {
  v = fmaxf(v, __shfl_xor(v, 1)); v = fmaxf(v, __shfl_xor(v, 2));
  v = fmaxf(v, __shfl_xor(v, 4)); v = fmaxf(v, __shfl_xor(v, 8));
  return v;
}


__device__ __forceinline__ float red8_sum(float v) {
  v += __shfl_xor(v, 1); v += __shfl_xor(v, 2); v += __shfl_xor(v, 4);
  return v;
}
__device__ __forceinline__ float lo(unsigned u) { return __uint_as_float(u << 16); }
__device__ __forceinline__ float hi(unsigned u) { return __uint_as_float(u & 0xffff0000u); }

constexpr int K1_A_BYTES = 64 * 1280;
constexpr int K1_SLOT = 32 * 512;
constexpr int K1_LDS = K1_A_BYTES + 4 * K1_SLOT;

__global__ __launch_bounds__(512) void k1_qkv(const u16* __restrict__ x, const u16* __restrict__ wqkv,
                                              const u16* __restrict__ wphi, u16* __restrict__ phiq,
                                              float* __restrict__ kvp) {
  __shared__ __attribute__((aligned(16))) char smem[K1_LDS];
  char* sA = smem;
  char* sB = smem + K1_A_BYTES;
  const int tid = threadIdx.x, lane = tid & 63, w = tid >> 6;
  const int hg = blockIdx.x & 7, mt = blockIdx.x >> 3;
  const int m0 = mt * 64;
  const unsigned sA_u = lds_addr(smem);
  const int wu = __builtin_amdgcn_readfirstlane(w);

#pragma unroll 1
  for (int j = 0; j < 10; ++j) {
    const int pos = (wu * 10 + j) * 64 + lane;
    const int row = pos / 80, pc = pos % 80;
    glds16(x + (size_t)(m0 + row) * 640 + ((pc ^ (row & 15)) * 8), sA_u + (wu * 10 + j) * 1024);
  }
  int boff[2];
#pragma unroll
  for (int j = 0; j < 2; ++j) {
    const int r = (w * 2 + j) * 2 + (lane >> 5);
    const int pc = lane & 31;
    int u = (pc >> 1) ^ bswz(r);
    if (u == 15) u = 14;
    const int n8 = u * 2 + (pc & 1);
    boff[j] = r * 1920 + (n8 / 10) * 640 + hg * 80 + (n8 % 10) * 8;
  }
  const unsigned sB_u = sA_u + K1_A_BYTES + wu * 2048;
#pragma unroll 1
  for (int t = 0; t < 3; ++t) {
    glds16(wqkv + (size_t)t * 32 * 1920 + boff[0], sB_u + t * K1_SLOT);
    glds16(wqkv + (size_t)t * 32 * 1920 + boff[1], sB_u + t * K1_SLOT + 1024);
  }
  WAIT_V(6);
  __builtin_amdgcn_s_barrier();
  {
    char* rp = sA + (tid >> 3) * 1280 + (tid & 7) * 160;
    float s = 0.f;
#pragma unroll 1
    for (int i = 0; i < 10; ++i) {
      u32x4 v = *(const u32x4*)(rp + i * 16);
      s += (lo(v[0]) + hi(v[0])) + (lo(v[1]) + hi(v[1])) + (lo(v[2]) + hi(v[2])) + (lo(v[3]) + hi(v[3]));
    }
    const float mean = red8_sum(s) * (1.f / 640.f);
    float q = 0.f;
#pragma unroll 1
    for (int i = 0; i < 10; ++i) {
      u32x4 v = *(const u32x4*)(rp + i * 16);
#pragma unroll
      for (int e = 0; e < 4; ++e) { float a = lo(v[e]) - mean, b = hi(v[e]) - mean; q += a * a + b * b; }
    }
    const float rstd = rsqrtf(red8_sum(q) * (1.f / 640.f) + 1e-5f);
#pragma unroll 1
    for (int i = 0; i < 10; ++i) {
      u32x4 v = *(const u32x4*)(rp + i * 16);
#pragma unroll
      for (int e = 0; e < 4; ++e) v[e] = pack2((lo(v[e]) - mean) * rstd, (hi(v[e]) - mean) * rstd);
      *(u32x4*)(rp + i * 16) = v;
    }
  }
  asm volatile("s_waitcnt lgkmcnt(0)" ::: "memory");

  const int wm = w >> 2, wn = w & 3;
  f32x4 acc[2][4];
#pragma unroll
  for (int a = 0; a < 2; ++a)
#pragma unroll
    for (int b = 0; b < 4; ++b) acc[a][b] = (f32x4){0.f, 0.f, 0.f, 0.f};
  const int g4 = lane >> 4, q4 = (lane & 15) >> 2, p4 = lane & 3;
  const unsigned fz = q4 | ((g4 & 1) << 2);
  const int r0 = 8 * g4 + q4;
  const int arow = wm * 32 + (lane & 15);
#pragma unroll 1
  for (int t = 0; t < 20; ++t) {
    WAIT_V(4);
    BAR();
    {
      const int tn = (t + 3 < 20) ? t + 3 : 19;
      glds16(wqkv + (size_t)tn * 32 * 1920 + boff[0], sB_u + ((t + 3) & 3) * K1_SLOT);
      glds16(wqkv + (size_t)tn * 32 * 1920 + boff[1], sB_u + ((t + 3) & 3) * K1_SLOT + 1024);
    }
    const char* sBc = sB + (t & 3) * K1_SLOT + r0 * 512 + 8 * p4;
    const int c = t * 4 + g4;
    bf16x8 a0 = *(const bf16x8*)(sA + arow * 1280 + ((c ^ (arow & 15)) * 16));
    bf16x8 a1 = *(const bf16x8*)(sA + (arow + 16) * 1280 + ((c ^ (arow & 15)) * 16));
#pragma unroll
    for (int ni = 0; ni < 4; ++ni) {
      bf16x8 bfr = tr_read2(sBc, ((wn * 4 + ni) ^ fz) * 32, 4 * 512);
      acc[0][ni] = MFMA(a0, bfr, acc[0][ni]);
      acc[1][ni] = MFMA(a1, bfr, acc[1][ni]);
    }
  }
  WAIT_V(0);
  __syncthreads();

  u16* sQ = (u16*)smem;
  u16* sW = (u16*)(smem + 32768);
  float* sP = (float*)(smem + 40960);
  u16* sPK = (u16*)(smem + 83968);
  u16* sPQ = (u16*)(smem + 108544);
  {
    u16* qp = sQ + (wm * 32 + g4 * 4) * 256 + wn * 64 + (lane & 15);
#pragma unroll
    for (int mi = 0; mi < 2; ++mi)
#pragma unroll
      for (int ni = 0; ni < 4; ++ni)
#pragma unroll
        for (int j = 0; j < 4; ++j) qp[(mi * 16 + j) * 256 + ni * 16] = f2bf(acc[mi][ni][j]);
  }
#pragma unroll
  for (int i = tid; i < 4096; i += 512) {
    const int hh = i >> 10, d = (i >> 5) & 31, f = i & 31;
    sW[i] = (d < 20 && f < 20) ? wphi[(hg * 4 + hh) * 400 + d * 20 + f] : (u16)0;
  }
  __syncthreads();
  const int ph = w & 3, pp = w >> 2;
  {
    bf16x8 wf0 = tr_read2((const char*)sW, (ph * 32 + r0) * 64 + 8 * p4, 4 * 64);
    bf16x8 wf1 = tr_read2((const char*)sW, (ph * 32 + r0) * 64 + 32 + 8 * p4, 4 * 64);
#pragma unroll 1
    for (int mi = 0; mi < 4; ++mi) {
      const u16* ap = sQ + (mi * 16 + (lane & 15)) * 256 + pp * 80 + ph * 20 + 8 * g4;
      u32x2 a0 = *(const u32x2*)ap;
      u32x2 a1 = *(const u32x2*)(ap + 4);
      u32x4 av = {a0[0], a0[1], a1[0], a1[1]};
      bf16x8 af = __builtin_bit_cast(bf16x8, av);
      f32x4 z = {0.f, 0.f, 0.f, 0.f};
      f32x4 p0 = MFMA(af, wf0, z);
      f32x4 p1 = MFMA(af, wf1, z);
      float* pp_ = sP + ((pp * 4 + ph) * 64 + mi * 16 + g4 * 4) * 21 + (lane & 15);
#pragma unroll
      for (int j = 0; j < 4; ++j) {
        pp_[j * 21] = rbf(p0[j]);
        if ((lane & 15) < 4) pp_[j * 21 + 16] = rbf(p1[j]);
      }
    }
  }
  __syncthreads();
  {
    const float* pr = sP + tid * 21;
    float mx = pr[0], mn = pr[0];
#pragma unroll 1
    for (int f = 1; f < 20; ++f) { const float v = pr[f]; mx = fmaxf(mx, v); mn = fminf(mn, v); }
    float sp = 0.f, sn = 0.f;
#pragma unroll 1
    for (int f = 0; f < 20; ++f) { const float v = pr[f]; sp += __expf(v - mx); sn += __expf(mn - v); }
    const float ip = __builtin_amdgcn_rcpf(sp), in_ = __builtin_amdgcn_rcpf(sn);
    const int it = tid & 255;
    u16* dst = (tid < 256) ? (sPQ + it * 40) : (sPK + it * 48);
#pragma unroll 1
    for (int f = 0; f < 20; ++f) {
      const float v = pr[f];
      dst[f] = f2bf(__expf(v - mx) * ip);
      dst[20 + f] = f2bf(__expf(mn - v) * in_);
    }
  }
  __syncthreads();
#pragma unroll 1
  for (int i = tid; i < 4 * 320; i += 512) {
    const int hh = i / 320, c = i % 320;
    u32x4 v = *(const u32x4*)((const char*)sPQ + hh * 5120 + c * 16);
    *(u32x4*)((char*)(phiq + ((size_t)(hg * 4 + hh) * S_ + m0) * 40) + c * 16) = v;
  }
  {
    const int nt = pp;
    const bf16x8 ones = {0x3f80, 0x3f80, 0x3f80, 0x3f80, 0x3f80, 0x3f80, 0x3f80, 0x3f80};
    f32x4 kv[3], zz[3];
#pragma unroll
    for (int ft = 0; ft < 3; ++ft) { kv[ft] = (f32x4){0.f, 0.f, 0.f, 0.f}; zz[ft] = kv[ft]; }
#pragma unroll 1
    for (int ks = 0; ks < 2; ++ks) {
      const int kr = ks * 32 + r0;
      bf16x8 bfr = tr_read2((const char*)sQ, kr * 512 + (160 + ph * 20 + nt * 16 + 4 * p4) * 2, 4 * 512);
#pragma unroll
      for (int ft = 0; ft < 3; ++ft) {
        bf16x8 afr = tr_read2((const char*)sPK, (ph * 64 + kr) * 96 + ft * 32 + 8 * p4, 4 * 96);
        kv[ft] = MFMA(afr, bfr, kv[ft]);
        zz[ft] = MFMA(afr, ones, zz[ft]);
      }
    }
    float* o = kvp + ((size_t)(hg * 4 + ph) * 32 + mt) * 840 + g4 * 4 * 21;
    const int d = nt * 16 + (lane & 15);
    const bool isz = (d == 20);
    if (d < 21) {
#pragma unroll
      for (int ft = 0; ft < 3; ++ft)
#pragma unroll
        for (int j = 0; j < 4; ++j)
          if (ft < 2 || g4 < 2) o[(ft * 16 + j) * 21 + d] = isz ? zz[ft][j] : kv[ft][j];
    }
  }
}

__global__ __launch_bounds__(256) void k15_o(const u16* __restrict__ phiq, const float* __restrict__ kvp,
                                             u16* __restrict__ o) {
  __shared__ __attribute__((aligned(16))) u16 skv[64 * 32];
  __shared__ __attribute__((aligned(16))) u16 sO[256 * 24];
  const int lb = (blockIdx.x & 7) * 32 + (blockIdx.x >> 3);
  const int head = lb >> 3, rc = lb & 7;
  const int tid = threadIdx.x, lane = tid & 63, w = tid >> 6;
  const int g4 = lane >> 4, q4 = (lane & 15) >> 2, p4 = lane & 3;
#pragma unroll 1
  for (int i = tid; i < 2048; i += 256) skv[i] = 0;
  float s[4] = {0.f, 0.f, 0.f, 0.f};
  const float* p = kvp + (size_t)head * 32 * 840 + tid;
  const int e3 = (tid + 768 < 840) ? 768 : 0;
#pragma unroll
  for (int m = 0; m < 32; ++m) {
    s[0] += p[m * 840]; s[1] += p[m * 840 + 256]; s[2] += p[m * 840 + 512]; s[3] += p[m * 840 + e3];
  }
  __syncthreads();
#pragma unroll
  for (int k = 0; k < 4; ++k) {
    const int e = tid + 256 * k;
    if (e < 840) skv[(e / 21) * 32 + e % 21] = f2bf(s[k]);
  }
  __syncthreads();
  const int r0 = 8 * g4 + q4;
  bf16x8 b00 = tr_read2((const char*)skv, r0 * 64 + 8 * p4, 256);
  bf16x8 b01 = tr_read2((const char*)skv, r0 * 64 + 32 + 8 * p4, 256);
  bf16x8 b10 = tr_read2((const char*)skv, (32 + r0) * 64 + 8 * p4, 256);
  bf16x8 b11 = tr_read2((const char*)skv, (32 + r0) * 64 + 32 + 8 * p4, 256);
#pragma unroll
  for (int mi = 0; mi < 4; ++mi) {
    const u16* gp = phiq + ((size_t)head * S_ + rc * 256 + w * 64 + mi * 16 + (lane & 15)) * 40;
    bf16x8 a0 = *(const bf16x8*)(gp + 8 * g4);
    bf16x8 a1 = *(const bf16x8*)(gp + 32 - 8 * g4 * (g4 != 0));
    f32x4 c0 = {0.f, 0.f, 0.f, 0.f}, c1 = {0.f, 0.f, 0.f, 0.f};
    c0 = MFMA(a0, b00, c0); c0 = MFMA(a1, b10, c0);
    c1 = MFMA(a0, b01, c1); c1 = MFMA(a1, b11, c1);
    u16* op = sO + (w * 64 + mi * 16 + g4 * 4) * 24 + (lane & 15);
#pragma unroll
    for (int j = 0; j < 4; ++j) {
      float den = __shfl(c1[j], (lane & 48) | 4);
      den = rbf(rbf(den) + 1e-6f);
      const float inv = 1.f / den;
      op[j * 24] = f2bf(rbf(c0[j]) * inv);
      if ((lane & 15) < 4) op[j * 24 + 16] = f2bf(rbf(c1[j]) * inv);
    }
  }
  __syncthreads();
#pragma unroll 1
  for (int i = tid; i < 256 * 5; i += 256) {
    const int rl = i / 5, c = i % 5;
    u32x2 v = *(const u32x2*)((const char*)sO + rl * 48 + c * 8);
    *(u32x2*)((char*)(o + (size_t)(rc * 256 + rl) * 640 + head * 20) + c * 8) = v;
  }
}

constexpr int G2_SLOT = 8192 + 16384;
template <int KTOT, bool STATS>
__global__ __launch_bounds__(512) void k_gemm64x80(const u16* __restrict__ A, const u16* __restrict__ B,
                                                   const u16* __restrict__ res, u16* __restrict__ out,
                                                   float2* __restrict__ stats) {
  constexpr int NT = KTOT / 64;
  __shared__ __attribute__((aligned(16))) char smem[5 * G2_SLOT];
  const int tid = threadIdx.x, lane = tid & 63, w = tid >> 6;
  const int lb = (blockIdx.x & 7) * 32 + (blockIdx.x >> 3);
  const int nt = lb & 7, mt = lb >> 3;
  const int m0 = mt * 64, n0 = nt * 80;
  const u16* ap;
  const u16* bp[2];
  {
    const int row = w * 8 + (lane >> 3);
    ap = A + (size_t)(m0 + row) * KTOT + ((lane & 7) ^ (row & 7)) * 8;
  }
#pragma unroll
  for (int j = 0; j < 2; ++j) {
    const int r = (w * 2 + j) * 4 + (lane >> 4);
    const int pc = lane & 15;
    int u = (pc >> 1) ^ bswz(r);
    if (u > 4) u = 4;
    bp[j] = B + r * 640 + n0 + u * 16 + (pc & 1) * 8;
  }
  const unsigned sm_u = lds_addr(smem);
  const int wu = __builtin_amdgcn_readfirstlane(w);
  const int wm = w >> 2, wk = (w >> 1) & 1, wn = w & 1;
  f32x4 acc[2][3];
#pragma unroll
  for (int a = 0; a < 2; ++a)
#pragma unroll
    for (int b = 0; b < 3; ++b) acc[a][b] = (f32x4){0.f, 0.f, 0.f, 0.f};
  const int g4 = lane >> 4, q4 = (lane & 15) >> 2, p4 = lane & 3;
  const unsigned fz = q4 | ((g4 & 1) << 2);
  const int arow = wm * 32 + (lane & 15);
  const int aoff = arow * 128 + (((wk * 4 + g4) ^ (arow & 7)) * 16);
  const int boffl = 8192 + (wk * 32 + 8 * g4 + q4) * 256 + 8 * p4;
  unsigned rv5[5];
  {
    const unsigned* rp = (const unsigned*)(res + (size_t)(m0 + (tid >> 3)) * 640 + n0 + (tid & 7) * 10);
#pragma unroll
    for (int i = 0; i < 5; ++i) rv5[i] = rp[i];
  }
  asm volatile("s_waitcnt vmcnt(0)" ::: "memory");
  int slot = 0;
#pragma unroll 1
  for (int t = -4; t < NT; ++t) {
    if (t >= 0) { WAIT_V(9); BAR(); }
    {
      const int tn = (t + 4 < NT) ? t + 4 : NT - 1;
      int sl = slot + 4; if (sl >= 5) sl -= 5;
      const unsigned d = sm_u + sl * G2_SLOT;
      glds16(ap + tn * 64, d + wu * 1024);
      glds16(bp[0] + (size_t)tn * 64 * 640, d + 8192 + wu * 2048);
      glds16(bp[1] + (size_t)tn * 64 * 640, d + 8192 + wu * 2048 + 1024);
    }
    if (t >= 0) {
      const char* sc = smem + slot * G2_SLOT;
      bf16x8 a0 = *(const bf16x8*)(sc + aoff);
      bf16x8 a1 = *(const bf16x8*)(sc + aoff + 2048);
#pragma unroll
      for (int ni = 0; ni < 3; ++ni) {
        const int un = (wn * 3 + ni > 4) ? 4 : wn * 3 + ni;
        bf16x8 bfr = tr_read2(sc, boffl + (un ^ fz) * 32, 4 * 256);
        acc[0][ni] = MFMA(a0, bfr, acc[0][ni]);
        acc[1][ni] = MFMA(a1, bfr, acc[1][ni]);
      }
    }
    slot = (slot == 4) ? 0 : slot + 1;
  }
  WAIT_V(0);
  __syncthreads();
  {
    float* rp = (float*)smem + wk * 6400 + (wm * 32 + g4 * 4) * 100 + wn * 48 + (lane & 15);
#pragma unroll
    for (int mi = 0; mi < 2; ++mi)
#pragma unroll
      for (int ni = 0; ni < 3; ++ni)
#pragma unroll
        for (int j = 0; j < 4; ++j) rp[(mi * 16 + j) * 100 + ni * 16] = acc[mi][ni][j];
  }
  __syncthreads();
  {
    const int row = tid >> 3, cb = (tid & 7) * 10;
    const float* r0p = (const float*)smem + row * 100 + cb;
    unsigned* op = (unsigned*)(out + (size_t)(m0 + row) * 640 + n0 + cb);
    float s = 0.f, s2 = 0.f;
    float* wp = (float*)smem + row * 100 + cb;
#pragma unroll
    for (int i = 0; i < 5; ++i) {
      const unsigned rv = rv5[i];
      const float a = rbf(lo(rv) + rbf(r0p[2 * i] + r0p[6400 + 2 * i]));
      const float b = rbf(hi(rv) + rbf(r0p[2 * i + 1] + r0p[6400 + 2 * i + 1]));
      op[i] = (__float_as_uint(a) >> 16) | (__float_as_uint(b) & 0xffff0000u);
      if (STATS) { wp[2 * i] = a; wp[2 * i + 1] = b; s += a + b; }
    }
    if (STATS) {
      s = red8_sum(s);
      const float mu = s * (1.f / 80.f);
#pragma unroll 1
      for (int i = 0; i < 10; ++i) { const float d = wp[i] - mu; s2 += d * d; }
      s2 = red8_sum(s2);
      if ((tid & 7) == 0) stats[(size_t)(m0 + row) * 8 + nt] = make_float2(s, s2);
    }
  }
}

__global__ __launch_bounds__(512) void k3_ffn1(const u16* __restrict__ h, const u16* __restrict__ w1,
                                               const float2* __restrict__ stats, u16* __restrict__ hmid) {
  __shared__ __attribute__((aligned(16))) char smem[4 * 32768 + 2048];
  const int tid = threadIdx.x, lane = tid & 63, w = tid >> 6;
  const int lb = (blockIdx.x & 7) * 28 + (blockIdx.x >> 3);
  const int nt = lb % 14, mt = lb / 14;
  const int m0 = mt * 128, n0 = nt * 128;
  float* sMean = (float*)(smem + 131072);
  const u16* ap[2];
  const u16* bp[2];
#pragma unroll
  for (int j = 0; j < 2; ++j) {
    const int row = (w * 2 + j) * 8 + (lane >> 3);
    ap[j] = h + (size_t)(m0 + row) * 640 + ((lane & 7) ^ (row & 7)) * 8;
    const int r = (w * 2 + j) * 4 + (lane >> 4);
    const int pc = lane & 15;
    int col = n0 + ((pc >> 1) ^ bswz(r)) * 16 + (pc & 1) * 8;
    if (col >= F_) col = F_ - 8;
    bp[j] = w1 + r * F_ + col;
  }
  const unsigned sm_u = lds_addr(smem);
  const int wu = __builtin_amdgcn_readfirstlane(w);
  if (tid < 128) {
    const float2* sp = stats + (size_t)(m0 + tid) * 8;
    float2 sv[8];
#pragma unroll
    for (int i = 0; i < 8; ++i) sv[i] = sp[i];
    float tot = 0.f, m2 = 0.f;
#pragma unroll
    for (int i = 0; i < 8; ++i) { tot += sv[i].x; m2 += sv[i].y; }
    const float mu = tot * (1.f / 640.f);
#pragma unroll
    for (int i = 0; i < 8; ++i) { const float d = sv[i].x * (1.f / 80.f) - mu; m2 += 80.f * d * d; }
    sMean[tid] = mu;
    sMean[128 + tid] = rsqrtf(m2 * (1.f / 640.f) + 1e-5f);
  }
  asm volatile("s_waitcnt vmcnt(0) lgkmcnt(0)" ::: "memory");
  const int wm = w >> 1, wn = w & 1;
  const int g4 = lane >> 4, q4 = (lane & 15) >> 2, p4 = lane & 3;
  f32x4 acc[2][4], cs[4];
#pragma unroll
  for (int b = 0; b < 4; ++b) {
    acc[0][b] = (f32x4){0.f, 0.f, 0.f, 0.f};
    acc[1][b] = acc[0][b];
    cs[b] = acc[0][b];
  }
  const bf16x8 ones = {0x3f80, 0x3f80, 0x3f80, 0x3f80, 0x3f80, 0x3f80, 0x3f80, 0x3f80};
  const unsigned fz = q4 | ((g4 & 1) << 2);
  const int arow = wm * 32 + (lane & 15);
#pragma unroll 1
  for (int t = -3; t < 20; ++t) {
    if (t >= 0 && !(t & 1)) { WAIT_V(8); BAR(); }
    if (t < 0 || !(t & 1)) {
      const int st = (t < 0) ? t + 3 : (t >> 1) + 3;
      const int tn = (st < 10) ? st : 9;
      const unsigned d = sm_u + (st & 3) * 32768 + wu * 2048;
      glds16(ap[0] + tn * 64, d);
      glds16(ap[1] + tn * 64, d + 1024);
      glds16(bp[0] + (size_t)tn * 64 * F_, d + 16384);
      glds16(bp[1] + (size_t)tn * 64 * F_, d + 16384 + 1024);
    }
    if (t >= 0) {
      const int kk = t & 1;
      const char* sc = smem + ((t >> 1) & 3) * 32768;
      const int ao = arow * 128 + (((kk * 4 + g4) ^ (arow & 7)) * 16);
      bf16x8 a0 = *(const bf16x8*)(sc + ao);
      bf16x8 a1 = *(const bf16x8*)(sc + ao + 2048);
      const char* sb = sc + 16384 + (kk * 32 + 8 * g4 + q4) * 256 + 8 * p4;
#pragma unroll
      for (int ni = 0; ni < 4; ++ni) {
        bf16x8 bfr = tr_read2(sb, ((wn * 4 + ni) ^ fz) * 32, 4 * 256);
        acc[0][ni] = MFMA(a0, bfr, acc[0][ni]);
        acc[1][ni] = MFMA(a1, bfr, acc[1][ni]);
        cs[ni] = MFMA(ones, bfr, cs[ni]);
      }
    }
  }
  WAIT_V(0);
  __syncthreads();
  {
    float* rp = (float*)smem + (wm * 32 + g4 * 4) * 132 + wn * 64 + (lane & 15);
#pragma unroll
    for (int mi = 0; mi < 2; ++mi)
#pragma unroll
      for (int ni = 0; ni < 4; ++ni)
#pragma unroll
        for (int j = 0; j < 4; ++j) rp[(mi * 16 + j) * 132 + ni * 16] = acc[mi][ni][j];
    if (w < 2 && g4 == 0) {
#pragma unroll
      for (int ni = 0; ni < 4; ++ni) sMean[256 + wn * 64 + ni * 16 + lane] = cs[ni][0];
    }
  }
  __syncthreads();
#pragma unroll 1
  for (int i = tid; i < 128 * 16; i += 512) {
    const int r = i >> 4, c = i & 15;
    const float mu = sMean[r], rs = sMean[128 + r];
    const float* vp = (const float*)smem + r * 132 + c * 8;
    const float* cp = sMean + 256 + c * 8;
    u32x4 ov;
#pragma unroll
    for (int e = 0; e < 4; ++e) {
      const float a = rbf(rs * (vp[2 * e] - mu * cp[2 * e]));
      const float b = rbf(rs * (vp[2 * e + 1] - mu * cp[2 * e + 1]));
      ov[e] = pack2(a * __builtin_amdgcn_rcpf(1.f + __expf(-a)), b * __builtin_amdgcn_rcpf(1.f + __expf(-b)));
    }
    if (n0 + c * 8 < F_) *(u32x4*)(hmid + (size_t)(m0 + r) * F_ + n0 + c * 8) = ov;
  }
}

extern "C" void kernel_launch(void* const* d_in, const int* in_sizes, int n_in, void* d_out, int out_size,
                              void* d_ws, size_t ws_size, hipStream_t stream) {
  const u16* x = (const u16*)d_in[0];
  const u16* wqkv = (const u16*)d_in[1];
  const u16* wphi = (const u16*)d_in[2];
  const u16* wo = (const u16*)d_in[3];
  const u16* w1 = (const u16*)d_in[5];
  const u16* w2 = (const u16*)d_in[6];
  u16* out = (u16*)d_out;
  char* ws = (char*)d_ws;
  u16* phiq = (u16*)ws;
  float* kvp = (float*)(ws + (8 << 20));
  u16* o = (u16*)(ws + (16 << 20));
  u16* h = (u16*)(ws + (24 << 20));
  u16* hmid = (u16*)(ws + (32 << 20));
  float2* stats = (float2*)(ws + (48 << 20));

  k1_qkv<<<256, 512, 0, stream>>>(x, wqkv, wphi, phiq, kvp);
  k15_o<<<256, 256, 0, stream>>>(phiq, kvp, o);
  k_gemm64x80<640, true><<<256, 512, 0, stream>>>(o, wo, x, h, stats);
  k3_ffn1<<<16 * 14, 512, 0, stream>>>(h, w1, stats, hmid);
  k_gemm64x80<1728, false><<<256, 512, 0, stream>>>(hmid, w2, h, out, nullptr);
}
```

```cpp
#include <hip/hip_runtime.h>
#include <hip/hip_bf16.h>
#include <stdint.h>

typedef __attribute__((ext_vector_type(8))) short bf16x8;
typedef __attribute__((ext_vector_type(4))) short bf16x4;
typedef __attribute__((ext_vector_type(4))) float f32x4;
typedef __attribute__((ext_vector_type(4))) unsigned u32x4;
typedef __attribute__((ext_vector_type(2))) unsigned u32x2;
typedef unsigned short u16;

#define LDS_PTR(T, p) ((__attribute__((address_space(3))) T*)(p))
#define WAIT_V(n) asm volatile("s_waitcnt vmcnt(" #n ")" ::: "memory")
#define BAR() __builtin_amdgcn_s_barrier()
#define MFMA(a, b, c) __builtin_amdgcn_mfma_f32_16x16x32_bf16(a, b, c, 0, 0, 0)

constexpr int S_ = 2048, D_ = 640, H_ = 32, DH_ = 20, F_ = 1728;

__device__ __forceinline__ float bf2f(u16 v) { return __uint_as_float(((unsigned)v) << 16); }
__device__ __forceinline__ u16 f2bf(float f) {
  __bf16 b = (__bf16)f;
  return __builtin_bit_cast(u16, b);
}
__device__ __forceinline__ float rbf(float f) { return bf2f(f2bf(f)); }
__device__ __forceinline__ unsigned pack2(float a, float b) {
  return (unsigned)f2bf(a) | ((unsigned)f2bf(b) << 16);
}
__device__ __forceinline__ bf16x4 tr_read(const char* lds_base, unsigned off) {
  return __builtin_amdgcn_ds_read_tr16_b64_v4i16(LDS_PTR(bf16x4, lds_base + off));
}
__device__ __forceinline__ bf16x8 tr_read2(const char* lds_base, unsigned off, unsigned rowbytes4) {
  bf16x4 b0 = tr_read(lds_base, off);
  bf16x4 b1 = tr_read(lds_base, off + rowbytes4);
  return __builtin_shufflevector(b0, b1, 0, 1, 2, 3, 4, 5, 6, 7);
}
__device__ __forceinline__ void glds16(const u16* g, unsigned dst_uniform) {
  unsigned keep;
  asm volatile("s_mov_b32 %0, m0\n\ts_mov_b32 m0, %2\n\ts_nop 0\n\tglobal_load_lds_dwordx4 %1, off\n\ts_mov_b32 m0, %0"
               : "=&s"(keep) : "v"(g), "s"(dst_uniform) : "memory");
}
__device__ __forceinline__ unsigned lds_addr(const void* p) {
  return __builtin_amdgcn_readfirstlane((unsigned)(size_t)LDS_PTR(char, p));
}
__device__ __forceinline__ unsigned bswz(unsigned r) { return (r & 3) | (((r >> 3) & 1) << 2); }

__device__ __forceinline__ float red16_sum(float v) {
  v += __shfl_xor(v, 1); v += __shfl_xor(v, 2); v += __shfl_xor(v, 4); v += __shfl_xor(v, 8);
  return v;
}
__device__ __forceinline__ float red16_max(float v) {
  v = fmaxf(v, __shfl_xor(v, 1)); v = fmaxf(v, __shfl_xor(v, 2));
  v = fmaxf(v, __shfl_xor(v, 4)); v = fmaxf(v, __shfl_xor(v, 8));
  return v;
}


__device__ __forceinline__ float red8_sum(float v) {
  v += __shfl_xor(v, 1); v += __shfl_xor(v, 2); v += __shfl_xor(v, 4);
  return v;
}
__device__ __forceinline__ float lo(unsigned u) { return __uint_as_float(u << 16); }
__device__ __forceinline__ float hi(unsigned u) { return __uint_as_float(u & 0xffff0000u); }

constexpr int K1_A_BYTES = 64 * 1280;
constexpr int K1_SLOT = 32 * 512;
constexpr int K1_LDS = K1_A_BYTES + 4 * K1_SLOT;

__global__ __launch_bounds__(512) void k1_qkv(const u16* __restrict__ x, const u16* __restrict__ wqkv,
                                              const u16* __restrict__ wphi, u16* __restrict__ phiq,
                                              float* __restrict__ kvp) {
  __shared__ __attribute__((aligned(16))) char smem[K1_LDS];
  char* sA = smem;
  char* sB = smem + K1_A_BYTES;
  const int tid = threadIdx.x, lane = tid & 63, w = tid >> 6;
  const int hg = blockIdx.x & 7, mt = blockIdx.x >> 3;
  const int m0 = mt * 64;
  const unsigned sA_u = lds_addr(smem);
  const int wu = __builtin_amdgcn_readfirstlane(w);

#pragma unroll 1
  for (int j = 0; j < 10; ++j) {
    const int pos = (wu * 10 + j) * 64 + lane;
    const int row = pos / 80, pc = pos % 80;
    glds16(x + (size_t)(m0 + row) * 640 + ((pc ^ (row & 15)) * 8), sA_u + (wu * 10 + j) * 1024);
  }
  int boff[2];
#pragma unroll
  for (int j = 0; j < 2; ++j) {
    const int r = (w * 2 + j) * 2 + (lane >> 5);
    const int pc = lane & 31;
    int u = (pc >> 1) ^ bswz(r);
    if (u == 15) u = 14;
    const int n8 = u * 2 + (pc & 1);
    boff[j] = r * 1920 + (n8 / 10) * 640 + hg * 80 + (n8 % 10) * 8;
  }
  const unsigned sB_u = sA_u + K1_A_BYTES + wu * 2048;
#pragma unroll 1
  for (int t = 0; t < 3; ++t) {
    glds16(wqkv + (size_t)t * 32 * 1920 + boff[0], sB_u + t * K1_SLOT);
    glds16(wqkv + (size_t)t * 32 * 1920 + boff[1], sB_u + t * K1_SLOT + 1024);
  }
  WAIT_V(6);
  __builtin_amdgcn_s_barrier();
  {
    char* rp = sA + (tid >> 3) * 1280 + (tid & 7) * 160;
    u32x4 v[10];
#pragma unroll
    for (int i = 0; i < 10; ++i) v[i] = *(const u32x4*)(rp + i * 16);
    float s = 0.f;
#pragma unroll
    for (int i = 0; i < 10; ++i)
      s += (lo(v[i][0]) + hi(v[i][0])) + (lo(v[i][1]) + hi(v[i][1])) + (lo(v[i][2]) + hi(v[i][2])) + (lo(v[i][3]) + hi(v[i][3]));
    const float mean = red8_sum(s) * (1.f / 640.f);
    float q = 0.f;
#pragma unroll
    for (int i = 0; i < 10; ++i)
#pragma unroll
      for (int e = 0; e < 4; ++e) { float a = lo(v[i][e]) - mean, b = hi(v[i][e]) - mean; q += a * a + b * b; }
    const float rstd = rsqrtf(red8_sum(q) * (1.f / 640.f) + 1e-5f);
#pragma unroll
    for (int i = 0; i < 10; ++i) {
#pragma unroll
      for (int e = 0; e < 4; ++e) v[i][e] = pack2((lo(v[i][e]) - mean) * rstd, (hi(v[i][e]) - mean) * rstd);
      *(u32x4*)(rp + i * 16) = v[i];
    }
  }
  asm volatile("s_waitcnt lgkmcnt(0)" ::: "memory");

  const int wm = w >> 2, wn = w & 3;
  f32x4 acc[2][4];
#pragma unroll
  for (int a = 0; a < 2; ++a)
#pragma unroll
    for (int b = 0; b < 4; ++b) acc[a][b] = (f32x4){0.f, 0.f, 0.f, 0.f};
  const int g4 = lane >> 4, q4 = (lane & 15) >> 2, p4 = lane & 3;
  const unsigned fz = q4 | ((g4 & 1) << 2);
  const int r0 = 8 * g4 + q4;
  const int arow = wm * 32 + (lane & 15);
#pragma unroll 1
  for (int t = 0; t < 20; ++t) {
    WAIT_V(4);
    BAR();
    {
      const int tn = (t + 3 < 20) ? t + 3 : 19;
      glds16(wqkv + (size_t)tn * 32 * 1920 + boff[0], sB_u + ((t + 3) & 3) * K1_SLOT);
      glds16(wqkv + (size_t)tn * 32 * 1920 + boff[1], sB_u + ((t + 3) & 3) * K1_SLOT + 1024);
    }
    const char* sBc = sB + (t & 3) * K1_SLOT + r0 * 512 + 8 * p4;
    const int c = t * 4 + g4;
    bf16x8 a0 = *(const bf16x8*)(sA + arow * 1280 + ((c ^ (arow & 15)) * 16));
    bf16x8 a1 = *(const bf16x8*)(sA + (arow + 16) * 1280 + ((c ^ (arow & 15)) * 16));
#pragma unroll
    for (int ni = 0; ni < 4; ++ni) {
      bf16x8 bfr = tr_read2(sBc, ((wn * 4 + ni) ^ fz) * 32, 4 * 512);
      acc[0][ni] = MFMA(a0, bfr, acc[0][ni]);
      acc[1][ni] = MFMA(a1, bfr, acc[1][ni]);
    }
  }
  WAIT_V(0);
  __syncthreads();

  u16* sQ = (u16*)smem;
  u16* sW = (u16*)(smem + 32768);
  float* sP = (float*)(smem + 40960);
  u16* sPK = (u16*)(smem + 90112);
  u16* sPQ = (u16*)(smem + 114688);
  {
    u16* qp = sQ + (wm * 32 + g4 * 4) * 256 + wn * 64 + (lane & 15);
#pragma unroll
    for (int mi = 0; mi < 2; ++mi)
#pragma unroll
      for (int ni = 0; ni < 4; ++ni)
#pragma unroll
        for (int j = 0; j < 4; ++j) qp[(mi * 16 + j) * 256 + ni * 16] = f2bf(acc[mi][ni][j]);
  }
#pragma unroll
  for (int i = tid; i < 4096; i += 512) {
    const int hh = i >> 10, d = (i >> 5) & 31, f = i & 31;
    sW[i] = (d < 20 && f < 20) ? wphi[(hg * 4 + hh) * 400 + d * 20 + f] : (u16)0;
  }
  __syncthreads();
  const int ph = w & 3, pp = w >> 2;
  {
    bf16x8 wf0 = tr_read2((const char*)sW, (ph * 32 + r0) * 64 + 8 * p4, 4 * 64);
    bf16x8 wf1 = tr_read2((const char*)sW, (ph * 32 + r0) * 64 + 32 + 8 * p4, 4 * 64);
#pragma unroll
    for (int mi = 0; mi < 4; ++mi) {
      const u16* ap = sQ + (mi * 16 + (lane & 15)) * 256 + pp * 80 + ph * 20 + 8 * g4;
      u32x2 a0 = *(const u32x2*)ap;
      u32x2 a1 = *(const u32x2*)(ap + 4);
      u32x4 av = {a0[0], a0[1], a1[0], a1[1]};
      bf16x8 af = __builtin_bit_cast(bf16x8, av);
      f32x4 z = {0.f, 0.f, 0.f, 0.f};
      f32x4 p0 = MFMA(af, wf0, z);
      f32x4 p1 = MFMA(af, wf1, z);
      float* pp_ = sP + ((pp * 4 + ph) * 64 + mi * 16 + g4 * 4) * 24 + (lane & 15);
#pragma unroll
      for (int j = 0; j < 4; ++j) {
        pp_[j * 24] = rbf(p0[j]);
        if ((lane & 15) < 4) pp_[j * 24 + 16] = rbf(p1[j]);
      }
    }
  }
  __syncthreads();
  {
    float pv[20];
#pragma unroll
    for (int i = 0; i < 5; ++i) {
      f32x4 t4 = *(const f32x4*)(sP + tid * 24 + i * 4);
      pv[4 * i] = t4[0]; pv[4 * i + 1] = t4[1]; pv[4 * i + 2] = t4[2]; pv[4 * i + 3] = t4[3];
    }
    float mx = pv[0], mn = pv[0];
#pragma unroll
    for (int f = 1; f < 20; ++f) { mx = fmaxf(mx, pv[f]); mn = fminf(mn, pv[f]); }
    float en[20], sp = 0.f, sn = 0.f;
#pragma unroll
    for (int f = 0; f < 20; ++f) { en[f] = __expf(mn - pv[f]); pv[f] = __expf(pv[f] - mx); sp += pv[f]; sn += en[f]; }
    const float ip = __builtin_amdgcn_rcpf(sp), in_ = __builtin_amdgcn_rcpf(sn);
    const int it = tid & 255;
    unsigned* dst = (unsigned*)((tid < 256) ? (sPQ + it * 40) : (sPK + it * 48));
#pragma unroll
    for (int f = 0; f < 10; ++f) {
      dst[f] = pack2(pv[2 * f] * ip, pv[2 * f + 1] * ip);
      dst[10 + f] = pack2(en[2 * f] * in_, en[2 * f + 1] * in_);
    }
  }
  __syncthreads();
#pragma unroll 1
  for (int i = tid; i < 4 * 320; i += 512) {
    const int hh = i / 320, c = i % 320;
    u32x4 v = *(const u32x4*)((const char*)sPQ + hh * 5120 + c * 16);
    *(u32x4*)((char*)(phiq + ((size_t)(hg * 4 + hh) * S_ + m0) * 40) + c * 16) = v;
  }
  {
    const int nt = pp;
    const bf16x8 ones = {0x3f80, 0x3f80, 0x3f80, 0x3f80, 0x3f80, 0x3f80, 0x3f80, 0x3f80};
    f32x4 kv[3], zz[3];
#pragma unroll
    for (int ft = 0; ft < 3; ++ft) { kv[ft] = (f32x4){0.f, 0.f, 0.f, 0.f}; zz[ft] = kv[ft]; }
#pragma unroll 1
    for (int ks = 0; ks < 2; ++ks) {
      const int kr = ks * 32 + r0;
      bf16x8 bfr = tr_read2((const char*)sQ, kr * 512 + (160 + ph * 20 + nt * 16 + 4 * p4) * 2, 4 * 512);
#pragma unroll
      for (int ft = 0; ft < 3; ++ft) {
        bf16x8 afr = tr_read2((const char*)sPK, (ph * 64 + kr) * 96 + ft * 32 + 8 * p4, 4 * 96);
        kv[ft] = MFMA(afr, bfr, kv[ft]);
        zz[ft] = MFMA(afr, ones, zz[ft]);
      }
    }
    float* o = kvp + ((size_t)(hg * 4 + ph) * 32 + mt) * 840 + g4 * 4 * 21;
    const int d = nt * 16 + (lane & 15);
    const bool isz = (d == 20);
    if (d < 21) {
#pragma unroll
      for (int ft = 0; ft < 3; ++ft)
#pragma unroll
        for (int j = 0; j < 4; ++j)
          if (ft < 2 || g4 < 2) o[(ft * 16 + j) * 21 + d] = isz ? zz[ft][j] : kv[ft][j];
    }
  }
}

__global__ __launch_bounds__(256) void k15_o(const u16* __restrict__ phiq, const float* __restrict__ kvp,
                                             u16* __restrict__ o) {
  __shared__ __attribute__((aligned(16))) u16 skv[64 * 32];
  __shared__ __attribute__((aligned(16))) u16 sO[256 * 24];
  const int lb = (blockIdx.x & 7) * 32 + (blockIdx.x >> 3);
  const int head = lb >> 3, rc = lb & 7;
  const int tid = threadIdx.x, lane = tid & 63, w = tid >> 6;
  const int g4 = lane >> 4, q4 = (lane & 15) >> 2, p4 = lane & 3;
#pragma unroll 1
  for (int i = tid; i < 2048; i += 256) skv[i] = 0;
  float s[4] = {0.f, 0.f, 0.f, 0.f};
  const float* p = kvp + (size_t)head * 32 * 840 + tid;
  const int e3 = (tid + 768 < 840) ? 768 : 0;
#pragma unroll
  for (int m = 0; m < 32; ++m) {
    s[0] += p[m * 840]; s[1] += p[m * 840 + 256]; s[2] += p[m * 840 + 512]; s[3] += p[m * 840 + e3];
  }
  __syncthreads();
#pragma unroll
  for (int k = 0; k < 4; ++k) {
    const int e = tid + 256 * k;
    if (e < 840) skv[(e / 21) * 32 + e % 21] = f2bf(s[k]);
  }
  __syncthreads();
  const int r0 = 8 * g4 + q4;
  bf16x8 b00 = tr_read2((const char*)skv, r0 * 64 + 8 * p4, 256);
  bf16x8 b01 = tr_read2((const char*)skv, r0 * 64 + 32 + 8 * p4, 256);
  bf16x8 b10 = tr_read2((const char*)skv, (32 + r0) * 64 + 8 * p4, 256);
  bf16x8 b11 = tr_read2((const char*)skv, (32 + r0) * 64 + 32 + 8 * p4, 256);
#pragma unroll
  for (int mi = 0; mi < 4; ++mi) {
    const u16* gp = phiq + ((size_t)head * S_ + rc * 256 + w * 64 + mi * 16 + (lane & 15)) * 40;
    bf16x8 a0 = *(const bf16x8*)(gp + 8 * g4);
    bf16x8 a1 = *(const bf16x8*)(gp + 32 - 8 * g4 * (g4 != 0));
    f32x4 c0 = {0.f, 0.f, 0.f, 0.f}, c1 = {0.f, 0.f, 0.f, 0.f};
    c0 = MFMA(a0, b00, c0); c0 = MFMA(a1, b10, c0);
    c1 = MFMA(a0, b01, c1); c1 = MFMA(a1, b11, c1);
    u16* op = sO + (w * 64 + mi * 16 + g4 * 4) * 24 + (lane & 15);
#pragma unroll
    for (int j = 0; j < 4; ++j) {
      float den = __shfl(c1[j], (lane & 48) | 4);
      den = rbf(rbf(den) + 1e-6f);
      const float inv = 1.f / den;
      op[j * 24] = f2bf(rbf(c0[j]) * inv);
      if ((lane & 15) < 4) op[j * 24 + 16] = f2bf(rbf(c1[j]) * inv);
    }
  }
  __syncthreads();
#pragma unroll 1
  for (int i = tid; i < 256 * 5; i += 256) {
    const int rl = i / 5, c = i % 5;
    u32x2 v = *(const u32x2*)((const char*)sO + rl * 48 + c * 8);
    *(u32x2*)((char*)(o + (size_t)(rc * 256 + rl) * 640 + head * 20) + c * 8) = v;
  }
}

constexpr int G2_SLOT = 8192 + 16384;
template <int KTOT, bool STATS>
__global__ __launch_bounds__(512) void k_gemm64x80(const u16* __restrict__ A, const u16* __restrict__ B,
                                                   const u16* __restrict__ res, u16* __restrict__ out,
                                                   float2* __restrict__ stats) {
  constexpr int NT = KTOT / 64;
  __shared__ __attribute__((aligned(16))) char smem[5 * G2_SLOT];
  const int tid = threadIdx.x, lane = tid & 63, w = tid >> 6;
  const int lb = (blockIdx.x & 7) * 32 + (blockIdx.x >> 3);
  const int nt = lb & 7, mt = lb >> 3;
  const int m0 = mt * 64, n0 = nt * 80;
  const u16* ap;
  const u16* bp[2];
  {
    const int row = w * 8 + (lane >> 3);
    ap = A + (size_t)(m0 + row) * KTOT + ((lane & 7) ^ (row & 7)) * 8;
  }
#pragma unroll
  for (int j = 0; j < 2; ++j) {
    const int r = (w * 2 + j) * 4 + (lane >> 4);
    const int pc = lane & 15;
    int u = (pc >> 1) ^ bswz(r);
    if (u > 4) u = 4;
    bp[j] = B + r * 640 + n0 + u * 16 + (pc & 1) * 8;
  }
  const unsigned sm_u = lds_addr(smem);
  const int wu = __builtin_amdgcn_readfirstlane(w);
  const int wm = w >> 2, wk = (w >> 1) & 1, wn = w & 1;
  f32x4 acc[2][3];
#pragma unroll
  for (int a = 0; a < 2; ++a)
#pragma unroll
    for (int b = 0; b < 3; ++b) acc[a][b] = (f32x4){0.f, 0.f, 0.f, 0.f};
  const int g4 = lane >> 4, q4 = (lane & 15) >> 2, p4 = lane & 3;
  const unsigned fz = q4 | ((g4 & 1) << 2);
  const int arow = wm * 32 + (lane & 15);
  const int aoff = arow * 128 + (((wk * 4 + g4) ^ (arow & 7)) * 16);
  const int boffl = 8192 + (wk * 32 + 8 * g4 + q4) * 256 + 8 * p4;
  unsigned rv5[5];
  {
    const unsigned* rp = (const unsigned*)(res + (size_t)(m0 + (tid >> 3)) * 640 + n0 + (tid & 7) * 10);
#pragma unroll
    for (int i = 0; i < 5; ++i) rv5[i] = rp[i];
  }
  asm volatile("s_waitcnt vmcnt(0)" ::: "memory");
  int slot = 0;
#pragma unroll 1
  for (int t = -4; t < NT; ++t) {
    if (t >= 0) { WAIT_V(9); BAR(); }
    {
      const int tn = (t + 4 < NT) ? t + 4 : NT - 1;
      int sl = slot + 4; if (sl >= 5) sl -= 5;
      const unsigned d = sm_u + sl * G2_SLOT;
      glds16(ap + tn * 64, d + wu * 1024);
      glds16(bp[0] + (size_t)tn * 64 * 640, d + 8192 + wu * 2048);
      glds16(bp[1] + (size_t)tn * 64 * 640, d + 8192 + wu * 2048 + 1024);
    }
    if (t >= 0) {
      const char* sc = smem + slot * G2_SLOT;
      bf16x8 a0 = *(const bf16x8*)(sc + aoff);
      bf16x8 a1 = *(const bf16x8*)(sc + aoff + 2048);
#pragma unroll
      for (int ni = 0; ni < 3; ++ni) {
        const int un = (wn * 3 + ni > 4) ? 4 : wn * 3 + ni;
        bf16x8 bfr = tr_read2(sc, boffl + (un ^ fz) * 32, 4 * 256);
        acc[0][ni] = MFMA(a0, bfr, acc[0][ni]);
        acc[1][ni] = MFMA(a1, bfr, acc[1][ni]);
      }
    }
    slot = (slot == 4) ? 0 : slot + 1;
  }
  WAIT_V(0);
  __syncthreads();
  {
    float* rp = (float*)smem + wk * 6400 + (wm * 32 + g4 * 4) * 100 + wn * 48 + (lane & 15);
#pragma unroll
    for (int mi = 0; mi < 2; ++mi)
#pragma unroll
      for (int ni = 0; ni < 3; ++ni)
#pragma unroll
        for (int j = 0; j < 4; ++j) rp[(mi * 16 + j) * 100 + ni * 16] = acc[mi][ni][j];
  }
  __syncthreads();
  {
    const int row = tid >> 3, cb = (tid & 7) * 10;
    const float* r0p = (const float*)smem + row * 100 + cb;
    unsigned* op = (unsigned*)(out + (size_t)(m0 + row) * 640 + n0 + cb);
    float s = 0.f, s2 = 0.f;
    float* wp = (float*)smem + row * 100 + cb;
#pragma unroll
    for (int i = 0; i < 5; ++i) {
      const unsigned rv = rv5[i];
      const float a = rbf(lo(rv) + rbf(r0p[2 * i] + r0p[6400 + 2 * i]));
      const float b = rbf(hi(rv) + rbf(r0p[2 * i + 1] + r0p[6400 + 2 * i + 1]));
      op[i] = (__float_as_uint(a) >> 16) | (__float_as_uint(b) & 0xffff0000u);
      if (STATS) { wp[2 * i] = a; wp[2 * i + 1] = b; s += a + b; }
    }
    if (STATS) {
      s = red8_sum(s);
      const float mu = s * (1.f / 80.f);
#pragma unroll 1
      for (int i = 0; i < 10; ++i) { const float d = wp[i] - mu; s2 += d * d; }
      s2 = red8_sum(s2);
      if ((tid & 7) == 0) stats[(size_t)(m0 + row) * 8 + nt] = make_float2(s, s2);
    }
  }
}

__global__ __launch_bounds__(512) void k3_ffn1(const u16* __restrict__ h, const u16* __restrict__ w1,
                                               const float2* __restrict__ stats, u16* __restrict__ hmid) {
  __shared__ __attribute__((aligned(16))) char smem[4 * 32768 + 2048];
  const int tid = threadIdx.x, lane = tid & 63, w = tid >> 6;
  const int lb = (blockIdx.x & 7) * 28 + (blockIdx.x >> 3);
  const int nt = lb % 14, mt = lb / 14;
  const int m0 = mt * 128, n0 = nt * 128;
  float* sMean = (float*)(smem + 131072);
  const u16* ap[2];
  const u16* bp[2];
#pragma unroll
  for (int j = 0; j < 2; ++j) {
    const int row = (w * 2 + j) * 8 + (lane >> 3);
    ap[j] = h + (size_t)(m0 + row) * 640 + ((lane & 7) ^ (row & 7)) * 8;
    const int r = (w * 2 + j) * 4 + (lane >> 4);
    const int pc = lane & 15;
    int col = n0 + ((pc >> 1) ^ bswz(r)) * 16 + (pc & 1) * 8;
    if (col >= F_) col = F_ - 8;
    bp[j] = w1 + r * F_ + col;
  }
  const unsigned sm_u = lds_addr(smem);
  const int wu = __builtin_amdgcn_readfirstlane(w);
  if (tid < 128) {
    const float2* sp = stats + (size_t)(m0 + tid) * 8;
    float2 sv[8];
#pragma unroll
    for (int i = 0; i < 8; ++i) sv[i] = sp[i];
    float tot = 0.f, m2 = 0.f;
#pragma unroll
    for (int i = 0; i < 8; ++i) { tot += sv[i].x; m2 += sv[i].y; }
    const float mu = tot * (1.f / 640.f);
#pragma unroll
    for (int i = 0; i < 8; ++i) { const float d = sv[i].x * (1.f / 80.f) - mu; m2 += 80.f * d * d; }
    sMean[tid] = mu;
    sMean[128 + tid] = rsqrtf(m2 * (1.f / 640.f) + 1e-5f);
  }
  asm volatile("s_waitcnt vmcnt(0) lgkmcnt(0)" ::: "memory");
  const int wm = w >> 1, wn = w & 1;
  const int g4 = lane >> 4, q4 = (lane & 15) >> 2, p4 = lane & 3;
  f32x4 acc[2][4], cs[4];
#pragma unroll
  for (int b = 0; b < 4; ++b) {
    acc[0][b] = (f32x4){0.f, 0.f, 0.f, 0.f};
    acc[1][b] = acc[0][b];
    cs[b] = acc[0][b];
  }
  const bf16x8 ones = {0x3f80, 0x3f80, 0x3f80, 0x3f80, 0x3f80, 0x3f80, 0x3f80, 0x3f80};
  const unsigned fz = q4 | ((g4 & 1) << 2);
  const int arow = wm * 32 + (lane & 15);
#pragma unroll 1
  for (int t = -3; t < 20; ++t) {
    if (t >= 0 && !(t & 1)) { WAIT_V(8); BAR(); }
    if (t < 0 || !(t & 1)) {
      const int st = (t < 0) ? t + 3 : (t >> 1) + 3;
      const int tn = (st < 10) ? st : 9;
      const unsigned d = sm_u + (st & 3) * 32768 + wu * 2048;
      glds16(ap[0] + tn * 64, d);
      glds16(ap[1] + tn * 64, d + 1024);
      glds16(bp[0] + (size_t)tn * 64 * F_, d + 16384);
      glds16(bp[1] + (size_t)tn * 64 * F_, d + 16384 + 1024);
    }
    if (t >= 0) {
      const int kk = t & 1;
      const char* sc = smem + ((t >> 1) & 3) * 32768;
      const int ao = arow * 128 + (((kk * 4 + g4) ^ (arow & 7)) * 16);
      bf16x8 a0 = *(const bf16x8*)(sc + ao);
      bf16x8 a1 = *(const bf16x8*)(sc + ao + 2048);
      const char* sb = sc + 16384 + (kk * 32 + 8 * g4 + q4) * 256 + 8 * p4;
#pragma unroll
      for (int ni = 0; ni < 4; ++ni) {
        bf16x8 bfr = tr_read2(sb, ((wn * 4 + ni) ^ fz) * 32, 4 * 256);
        acc[0][ni] = MFMA(a0, bfr, acc[0][ni]);
        acc[1][ni] = MFMA(a1, bfr, acc[1][ni]);
        cs[ni] = MFMA(ones, bfr, cs[ni]);
      }
    }
  }
  WAIT_V(0);
  __syncthreads();
  {
    float* rp = (float*)smem + (wm * 32 + g4 * 4) * 132 + wn * 64 + (lane & 15);
#pragma unroll
    for (int mi = 0; mi < 2; ++mi)
#pragma unroll
      for (int ni = 0; ni < 4; ++ni)
#pragma unroll
        for (int j = 0; j < 4; ++j) rp[(mi * 16 + j) * 132 + ni * 16] = acc[mi][ni][j];
    if (w < 2 && g4 == 0) {
#pragma unroll
      for (int ni = 0; ni < 4; ++ni) sMean[256 + wn * 64 + ni * 16 + lane] = cs[ni][0];
    }
  }
  __syncthreads();
#pragma unroll 1
  for (int i = tid; i < 128 * 16; i += 512) {
    const int r = i >> 4, c = i & 15;
    const float mu = sMean[r], rs = sMean[128 + r];
    const float* vp = (const float*)smem + r * 132 + c * 8;
    const float* cp = sMean + 256 + c * 8;
    u32x4 ov;
#pragma unroll
    for (int e = 0; e < 4; ++e) {
      const float a = rbf(rs * (vp[2 * e] - mu * cp[2 * e]));
      const float b = rbf(rs * (vp[2 * e + 1] - mu * cp[2 * e + 1]));
      ov[e] = pack2(a * __builtin_amdgcn_rcpf(1.f + __expf(-a)), b * __builtin_amdgcn_rcpf(1.f + __expf(-b)));
    }
    if (n0 + c * 8 < F_) *(u32x4*)(hmid + (size_t)(m0 + r) * F_ + n0 + c * 8) = ov;
  }
}

extern "C" void kernel_launch(void* const* d_in, const int* in_sizes, int n_in, void* d_out, int out_size,
                              void* d_ws, size_t ws_size, hipStream_t stream) {
  const u16* x = (const u16*)d_in[0];
  const u16* wqkv = (const u16*)d_in[1];
  const u16* wphi = (const u16*)d_in[2];
  const u16* wo = (const u16*)d_in[3];
  const u16* w1 = (const u16*)d_in[5];
  const u16* w2 = (const u16*)d_in[6];
  u16* out = (u16*)d_out;
  char* ws = (char*)d_ws;
  u16* phiq = (u16*)ws;
  float* kvp = (float*)(ws + (8 << 20));
  u16* o = (u16*)(ws + (16 << 20));
  u16* h = (u16*)(ws + (24 << 20));
  u16* hmid = (u16*)(ws + (32 << 20));
  float2* stats = (float2*)(ws + (48 << 20));

  k1_qkv<<<256, 512, 0, stream>>>(x, wqkv, wphi, phiq, kvp);
  k15_o<<<256, 256, 0, stream>>>(phiq, kvp, o);
  k_gemm64x80<640, true><<<256, 512, 0, stream>>>(o, wo, x, h, stats);
  k3_ffn1<<<16 * 14, 512, 0, stream>>>(h, w1, stats, hmid);
  k_gemm64x80<1728, false><<<256, 512, 0, stream>>>(hmid, w2, h, out, nullptr);
}
```
